```python
import torch

def _softmax(x, axis=-1):
    return torch.softmax(x, dim=axis)
def _gelu(x):
    return torch.nn.functional.gelu(x, approximate="tanh")
def _silu(x):
    return torch.nn.functional.silu(x)
def _sigmoid(x):
    return torch.sigmoid(x)
def _layernorm(x, axis=-1, eps=1e-5):
    m = x.mean(dim=axis, keepdim=True)
    v = x.var(dim=axis, keepdim=True, unbiased=False)
    return (x - m) / torch.sqrt(v + eps)
def _scatter_add(tgt, idx, src, axis=0):
    return tgt.clone().index_add_(axis, idx, src)
def _segment_sum(src, seg, n):
    out = torch.zeros((n,) + src.shape[1:], dtype=src.dtype)
    return out.index_add_(0, seg, src)
def _pad(x, pw):
    flat = tuple(v for pair in reversed(pw) for v in pair)
    return torch.nn.functional.pad(x, flat)

kernel_name = 'standalone__deep_cross_wide_deep__recsys'

def setup_inputs():

    _w = (lambda a: (lambda t: t.to(torch.bfloat16) if t.is_floating_point() else t)(torch.as_tensor(a)))
    B, F, D = 16384, 100, 32
    V, Nd = min(1000, 100000), 13
    Din = F * D
    H = 4 * D
    return {
        "idx": _w((lambda lo, hi, *s: torch.randint(lo, hi, s))(0, V, B, F).to(torch.int64)),
        "table": _w((lambda *s: torch.randn(*s, dtype=torch.bfloat16) * 1.6)(V, D) * 0.05),
        "x_wide": _w((lambda *s: torch.randn(*s, dtype=torch.bfloat16) * 1.6)(B, Nd)),
        "w_wide": _w((lambda *s: torch.randn(*s, dtype=torch.bfloat16) * 1.6)(Nd) * 0.05),
        "b_wide": _w((lambda *s: torch.randn(*s, dtype=torch.bfloat16) * 1.6)(1) * 0.01),
        "W1": _w((lambda *s: torch.randn(*s, dtype=torch.bfloat16) * 1.6)(Din, H) * (2.0 / Din) ** 0.5),
        "b1": _w((lambda *s: torch.randn(*s, dtype=torch.bfloat16) * 1.6)(H) * 0.01),
        "W2": _w((lambda *s: torch.randn(*s, dtype=torch.bfloat16) * 1.6)(H) * (1.0 / H) ** 0.5),
        "b2": _w((lambda *s: torch.randn(*s, dtype=torch.bfloat16) * 1.6)(1) * 0.01),
    }

def reference(idx, table, x_wide, w_wide, b_wide, W1, b1, W2, b2):
    emb = table[idx]
    deep_in = emb.reshape(emb.shape[0], -1)
    h = deep_in @ W1 + b1
    h = h * (lambda x, dt: x.to(dt))(h > 0, h.dtype)
    deep_logit = h @ W2 + b2
    wide_logit = x_wide @ w_wide + b_wide
    return _sigmoid(wide_logit + deep_logit)
```

```cpp
#include <hip/hip_runtime.h>
#include <hip/hip_bf16.h>
#include <stdint.h>


typedef __attribute__((ext_vector_type(8))) short bf16x8;
typedef __attribute__((ext_vector_type(4))) float f32x4;
typedef __attribute__((ext_vector_type(4))) unsigned u32x4;

constexpr int NB = 16384, NF = 100, ND = 32, NV = 1000, NW = 13, NH = 128;
constexpr int ROWS = 64;
constexpr int TBL_BLKS = 63;
constexpr int TBL_BYTES = TBL_BLKS * 1024;
constexpr int SCR_BYTES = 4 * 3 * 8 * 1024;
constexpr int LDS_BYTES = TBL_BYTES + SCR_BYTES;

__device__ __forceinline__ float bf2f(unsigned short x) { return __uint_as_float(((unsigned)x) << 16); }

__global__ __launch_bounds__(256, 1) void wd_kernel(
    const int* __restrict__ idx, const unsigned short* __restrict__ table,
    const unsigned short* __restrict__ x_wide, const unsigned short* __restrict__ w_wide,
    const unsigned short* __restrict__ b_wide, const unsigned short* __restrict__ W1,
    const unsigned short* __restrict__ b1, const unsigned short* __restrict__ W2,
    const unsigned short* __restrict__ b2, unsigned short* __restrict__ out) {
  extern __shared__ __attribute__((aligned(16))) char lds[];
  const int tid = threadIdx.x;
  const int lane = tid & 63;
  const int w = __builtin_amdgcn_readfirstlane(tid >> 6);
  const int r = lane & 15, q = lane >> 4;
  const int blk = blockIdx.x;
  const int row0 = blk * ROWS;
  const int rot = (blk >> 3) % 25;

  {
    const char* tsrc = (const char*)table;
    for (int b = w; b < TBL_BLKS; b += 4) {
      int v = b * 16 + r;
      v = v < NV ? v : NV - 1;
      __builtin_amdgcn_global_load_lds((const unsigned*)(tsrc + v * 64 + q * 16),
                                       (unsigned*)(lds + b * 1024), 16, 0, 0);
    }
    const char* isrc = (const char*)(idx + (size_t)row0 * NF);
    for (int b = w; b < 25; b += 4) {
      __builtin_amdgcn_global_load_lds((const unsigned*)(isrc + b * 1024 + lane * 16),
                                       (unsigned*)(lds + TBL_BYTES + b * 1024), 16, 0, 0);
    }
  }

  const __amdgpu_buffer_rsrc_t wrs =
      __builtin_amdgcn_make_buffer_rsrc((void*)W1, 0, NF * ND * NH * 2, 0x00020000);
  const int wvoff = q * 8192 + r * 16;

  auto gphys = [&](int slot) { int g = slot + rot; return g >= 25 ? g - 25 : g; };

  u32x4 raw[4][8];
  auto load_step = [&](int c, int g, int cc) {
    const int soff = g * 32768 + cc * 2048;
#pragma unroll
    for (int j = 0; j < 8; ++j)
      raw[c][j] = __builtin_amdgcn_raw_buffer_load_b128(wrs, wvoff + j * 256, soff, 0);
  };
  {
    const int g0 = gphys(6 * w);
#pragma unroll
    for (int c = 0; c < 4; ++c) load_step(c, g0, c);
  }

  f32x4 acc[4][8];
#pragma unroll
  for (int m = 0; m < 4; ++m)
#pragma unroll
    for (int t = 0; t < 8; ++t) acc[m][t] = (f32x4){0.f, 0.f, 0.f, 0.f};

  __builtin_amdgcn_s_waitcnt(0x8F70);
  __builtin_amdgcn_s_barrier();

  int aaddr[7][4];
#pragma unroll
  for (int s = 0; s < 7; ++s) {
    const int g = gphys(s < 6 ? 6 * w + s : 24);
#pragma unroll
    for (int m = 0; m < 4; ++m) {
      int v = *(const int*)(lds + TBL_BYTES + ((16 * m + r) * NF + 4 * g + q) * 4);
      aaddr[s][m] = (v >> 4) * 768 + (v << 4);
    }
  }
  __builtin_amdgcn_s_waitcnt(0xC07F);
  __builtin_amdgcn_s_barrier();

  auto compute_step = [&](int c, const int (&aa)[4], int coff) {
    bf16x8 a[4];
#pragma unroll
    for (int m = 0; m < 4; ++m) a[m] = *(const bf16x8*)(lds + aa[m] + coff);
#pragma unroll
    for (int t = 0; t < 8; ++t) {
      u32x4 bf;
      const unsigned sel = (t & 1) ? 0x07060302u : 0x05040100u;
#pragma unroll
      for (int p = 0; p < 4; ++p)
        bf[p] = __builtin_amdgcn_perm(raw[c][2 * p + 1][t >> 1], raw[c][2 * p][t >> 1], sel);
      bf16x8 wf = __builtin_bit_cast(bf16x8, bf);
#pragma unroll
      for (int m = 0; m < 4; ++m)
        acc[m][t] = __builtin_amdgcn_mfma_f32_16x16x32_bf16(wf, a[m], acc[m][t], 0, 0, 0);
    }
  };

#pragma unroll
  for (int s = 0; s < 6; ++s) {
#pragma unroll
    for (int c = 0; c < 4; ++c) {
      compute_step(c, aaddr[s], c * 256);
      if (s < 5) load_step(c, gphys(6 * w + s + 1), c);
      else if (c == 0) load_step(0, gphys(24), w);
    }
  }
  compute_step(0, aaddr[6], w * 256);

  char* scr = lds + TBL_BYTES;
#pragma unroll
  for (int m = 0; m < 4; ++m) {
    if (m != w) {
      const int sidx = (w - m - 1) & 3;
#pragma unroll
      for (int t = 0; t < 8; ++t)
        *(f32x4*)(scr + ((m * 3 + sidx) * 8 + t) * 1024 + lane * 16) = acc[m][t];
    }
  }
  f32x4 h[8];
#pragma unroll
  for (int t = 0; t < 8; ++t) {
    h[t] = w == 0 ? acc[0][t] : w == 1 ? acc[1][t] : w == 2 ? acc[2][t] : acc[3][t];
  }
  __syncthreads();
#pragma unroll
  for (int s = 0; s < 3; ++s)
#pragma unroll
    for (int t = 0; t < 8; ++t)
      h[t] += *(const f32x4*)(scr + ((w * 3 + s) * 8 + t) * 1024 + lane * 16);

  float part = 0.f;
#pragma unroll
  for (int reg = 0; reg < 4; ++reg) {
    const bf16x8 bb = *(const bf16x8*)(b1 + 32 * q + 8 * reg);
    const bf16x8 ww = *(const bf16x8*)(W2 + 32 * q + 8 * reg);
#pragma unroll
    for (int t = 0; t < 8; ++t) {
      float hv = h[t][reg] + bf2f((unsigned short)bb[t]);
      hv = fmaxf(hv, 0.f);
      part += hv * bf2f((unsigned short)ww[t]);
    }
  }
  const int row = row0 + 16 * w + r;
#pragma unroll
  for (int i = 0; i < 4; ++i) {
    const int f = q + 4 * i;
    if (f < NW) part += bf2f(x_wide[row * NW + f]) * bf2f(w_wide[f]);
  }
  part += __shfl_xor(part, 16);
  part += __shfl_xor(part, 32);
  if (q == 0) {
    const float z = part + bf2f(b2[0]) + bf2f(b_wide[0]);
    const float p = 1.f / (1.f + __expf(-z));
    __hip_bfloat16 o = __float2bfloat16(p);
    out[row] = *(unsigned short*)&o;
  }
}

extern "C" void kernel_launch(void* const* d_in, const int* in_sizes, int n_in, void* d_out,
                              int out_size, void* d_ws, size_t ws_size, hipStream_t stream) {
  (void)in_sizes; (void)n_in; (void)out_size; (void)d_ws; (void)ws_size;
  hipFuncSetAttribute((const void*)wd_kernel, hipFuncAttributeMaxDynamicSharedMemorySize, LDS_BYTES);
  wd_kernel<<<NB / ROWS, 256, LDS_BYTES, stream>>>(
      (const int*)d_in[0], (const unsigned short*)d_in[1], (const unsigned short*)d_in[2],
      (const unsigned short*)d_in[3], (const unsigned short*)d_in[4], (const unsigned short*)d_in[5],
      (const unsigned short*)d_in[6], (const unsigned short*)d_in[7], (const unsigned short*)d_in[8],
      (unsigned short*)d_out);
}
```

```cpp
#include <hip/hip_runtime.h>
#include <hip/hip_bf16.h>
#include <stdint.h>


constexpr int LDS_BYTES = 163840;

__global__ __launch_bounds__(256, 1) void wd_kernel(
    const int* __restrict__ idx, const unsigned short* __restrict__ table,
    const unsigned short* __restrict__ x_wide, const unsigned short* __restrict__ w_wide,
    const unsigned short* __restrict__ b_wide, const unsigned short* __restrict__ W1,
    const unsigned short* __restrict__ b1, const unsigned short* __restrict__ W2,
    const unsigned short* __restrict__ b2, unsigned short* __restrict__ out, unsigned long long* __restrict__ ws) {
  unsigned tid = threadIdx.x;
  const unsigned blk = blockIdx.x;
  const void* ka = (const void*)__builtin_amdgcn_kernarg_segment_ptr();
  asm volatile(
      "s_load_dwordx16 s[40:55], %[ka], 0x0\n"
      "s_load_dwordx4 s[56:59], %[ka], 0x40\n"
      "s_load_dwordx2 s[60:61], %[ka], 0x50\n"
      "s_getpc_b64 s[96:97]\n"
      "Lbeg%=:\n"
      "v_and_b32 v130, 63, v0\n"
      "v_lshrrev_b32 v129, 6, v0\n"
      "v_lshl_add_u32 v130, v130, 2, v129\n"
      "v_lshlrev_b32 v130, 7, v130\n"
      "v_min_u32 v130, Lend%=-Lbeg%=, v130\n"
      "global_load_dword v130, v130, s[96:97]\n"
      "s_mov_b32 s84, %[blk]\n"
      "v_and_b32 v240, 63, v0\n"
      "v_lshrrev_b32 v251, 6, v0\n"
      "v_and_b32 v241, 15, v0\n"
      "v_bfe_u32 v242, v0, 4, 2\n"
      "v_readfirstlane_b32 s76, v251\n"
      "s_mov_b32 s77, 0x05040100\n"
      "s_mov_b32 s78, 0x07060302\n"
      "s_lshr_b32 s86, s84, 3\n"
      "s_and_b32 s85, s86, 1\n"
      "s_lshr_b32 s90, s84, 4\n"
      "s_lshl_b32 s87, s90, 3\n"
      "s_and_b32 s91, s84, 7\n"
      "s_or_b32 s87, s87, s91\n"
      "s_mul_i32 s81, s76, 6\n"
      "s_add_u32 s81, s81, s90\n"
      "s_sub_u32 s91, s81, 25\n"
      "s_cmp_ge_u32 s81, 25\n"
      "s_cselect_b32 s81, s91, s81\n"
      "s_sub_u32 s99, s90, 1\n"
      "s_cmp_eq_u32 s90, 0\n"
      "s_cselect_b32 s99, 24, s99\n"
      "s_lshl_b32 s83, s99, 15\n"
      "s_lshl_b32 s90, s76, 11\n"
      "s_add_u32 s83, s83, s90\n"
      "v_lshlrev_b32 v248, 4, v240\n"
      "v_and_b32 v250, 1, v242\n"
      "v_lshrrev_b32 v249, 1, v240\n"
      "v_lshlrev_b32 v249, 6, v249\n"
      "v_and_b32 v252, 1, v240\n"
      "v_lshl_add_u32 v249, v252, 4, v249\n"
      "v_and_b32 v252, 7, v241\n"
      "v_lshlrev_b32 v252, 4, v252\n"
      "v_lshl_add_u32 v252, v242, 13, v252\n"
      "v_lshrrev_b32 v253, 3, v241\n"
      "v_lshl_add_u32 v252, v253, 10, v252\n"
      "s_lshl_b32 s90, s85, 7\n"
      "v_add_u32 v252, s90, v252\n"
      "v_lshlrev_b32 v253, 11, v250\n"
      "v_add_u32 v232, v252, v253\n"
      "v_sub_u32 v233, v252, v253\n"
      "v_lshlrev_b32 v235, 4, v250\n"
      "s_mov_b32 s66, 819200\n"
      "s_mov_b32 s67, 0x20000\n"
      "s_mov_b32 s74, 51200\n"
      "s_mov_b32 s75, 0x20000\n"
      "s_mov_b32 s70, 64000\n"
      "s_mov_b32 s71, 0x20000\n"
      "s_mul_i32 s90, s87, 51200\n"
      "s_mul_i32 s91, s76, 6144\n"
      "s_mul_i32 s92, s85, 25600\n"
      "s_sub_u32 s89, s91, s92\n"
      "s_add_u32 s89, s89, 25600\n"
      "s_add_u32 s92, s92, s91\n"
      "s_add_u32 s82, s91, 65536\n"
      "s_mov_b32 m0, s82\n"
      "s_lshl_b32 s93, s76, 10\n"
      "s_lshl_b32 s94, s84, 2\n"
      "s_add_u32 s94, s94, s76\n"
      "s_lshl_b32 s94, s94, 9\n"
      "s_xor_b32 s95, s94, 16384\n"
      "v_lshlrev_b32 v243, 3, v240\n"
      "v_mov_b32 v245, 0\n"
      "v_mov_b32 v254, v243\n"
      "s_mov_b64 exec, 0x20000\n"
      "v_mov_b32 v254, 256\n"
      "s_mov_b64 exec, -1\n"
      "s_lshl_b32 s79, s81, 15\n"
      "s_add_u32 s80, s79, 4096\n"
      "s_lshl_b32 s96, s87, 1\n"
      "s_add_u32 s96, s96, s85\n"
      "s_lshl_b32 s96, s96, 2\n"
      "s_add_u32 s87, s96, s76\n"
      "s_mul_i32 s96, s87, 416\n"
      "s_sub_u32 s96, s96, 512\n"
      "v_lshlrev_b32 v131, 4, v241\n"
      "s_mov_b32 exec_lo, 0\n"
      "v_add_u32 v131, s96, v248\n"
      "s_mov_b32 exec_lo, -1\n"
      "v_mov_b32 v146, 0\n"
      "s_lshl_b32 s62, s86, 2\n"
      "s_add_u32 s62, s62, s76\n"
      "s_lshl_b32 s63, s62, 13\n"
      "s_and_b32 s62, s62, 1\n"
      "s_lshl_b32 s62, s62, 11\n"
      "s_or_b32 s63, s63, s62\n"
      "v_lshlrev_b32 v129, 7, v240\n"
      "v_lshlrev_b32 v134, 1, v240\n"
      "s_waitcnt lgkmcnt(0)\n"
      "s_add_u32 s72, s40, s90\n"
      "s_addc_u32 s73, s41, 0\n"
      "s_and_b32 s73, s73, 0xffff\n"
      "s_mov_b32 s64, s50\n"
      "s_and_b32 s65, s51, 0xffff\n"
      "s_mov_b32 s68, s42\n"
      "s_and_b32 s69, s43, 0xffff\n"
      "s_add_u32 s36, s60, s94\n"
      "s_addc_u32 s37, s61, 0\n"
      "s_add_u32 s38, s60, s95\n"
      "s_addc_u32 s39, s61, 0\n"
      "s_mov_b64 exec, 0x3ffff\n"
      "global_load_dwordx2 v[244:245], v254, s[36:37] sc1\n"
      "s_mov_b64 exec, -1\n"
      "v_mov_b32 v132, s52\n"
      "v_mov_b32 v133, s53\n"
      "s_mov_b32 exec_lo, 0xffff0000\n"
      "v_mov_b32 v132, s54\n"
      "v_mov_b32 v133, s55\n"
      "s_mov_b32 exec_lo, 0\n"
      "v_mov_b32 v132, s44\n"
      "v_mov_b32 v133, s45\n"
      "s_mov_b32 exec_lo, -1\n"
      "s_mov_b32 exec_hi, 0x3ffffff\n"
      "v_add_co_u32 v132, vcc, v132, v131\n"
      "s_add_u32 m0, s93, 116736\n"
      "s_nop 0\n"
      "v_addc_co_u32 v133, vcc, 0, v133, vcc\n"
      "global_load_lds_dwordx4 v[132:133], off\n"
      "s_mov_b64 exec, -1\n"
      "v_mov_b32 v135, s47\n"
      "v_add_co_u32 v134, vcc, s46, v134\n"
      "s_nop 1\n"
      "v_addc_co_u32 v135, vcc, 0, v135, vcc\n"
      "s_mov_b64 exec, 0x2000\n"
      "v_mov_b32 v134, s48\n"
      "v_mov_b32 v135, s49\n"
      "s_mov_b64 exec, 0x4000\n"
      "v_mov_b32 v134, s56\n"
      "v_mov_b32 v135, s57\n"
      "s_mov_b64 exec, 0x7fff\n"
      "global_load_ushort v146, v[134:135], off\n"
      "s_mov_b64 exec, -1\n"
      "s_add_u32 m0, s82, 0\n"
      "s_nop 0\n"
      "buffer_load_dwordx4 v248, s[72:75], s92 offen offset:0 lds\n"
      "buffer_load_dwordx4 v248, s[72:75], s92 offen offset:1024 lds\n"
      "buffer_load_dwordx4 v248, s[72:75], s92 offen offset:2048 lds\n"
      "buffer_load_dwordx4 v248, s[72:75], s92 offen offset:3072 lds\n"
      "s_add_u32 m0, s82, 4096\n"
      "s_add_u32 s92, s92, 4096\n"
      "buffer_load_dwordx4 v248, s[72:75], s92 offen offset:0 lds\n"
      "buffer_load_dwordx4 v248, s[72:75], s92 offen offset:1024 lds\n"
      "s_cmp_lg_u32 s76, 3\n"
      "s_cbranch_scc1 Lw3a%=\n"
      "buffer_load_dwordx4 v248, s[72:75], s92 offen offset:2048 lds\n"
      "Lw3a%=:\n"
      "s_mov_b64 exec, 0xffff\n"
      "buffer_load_dword v129, v129, s[64:67], s63 offen\n"
      "s_mov_b64 exec, -1\n"
      "s_lshl_b32 s92, s76, 13\n"
      "s_lshl_b32 s98, s86, 10\n"
      "v_add_u32 v147, 32, v249\n"
      "s_and_b32 s98, s98, 0x1fff\n"
      "s_or_b32 s62, s92, s98\n"
      "s_mov_b32 m0, s62\n"
      "s_lshl_b32 s63, s62, 1\n"
      "buffer_load_dwordx4 v249, s[68:71], s63 offen lds\n"
      "v_mov_b32 v140, 0\n"
      "v_mov_b32 v141, 0\n"
      "v_mov_b32 v142, 0\n"
      "v_mov_b32 v143, 0\n"
      "s_nop 1\n"
      "v_mfma_f32_32x32x16_bf16 a[0:15], v[140:143], v[140:143], 0\n"
      "v_mfma_f32_32x32x16_bf16 a[16:31], v[140:143], v[140:143], 0\n"
      "v_mfma_f32_32x32x16_bf16 a[32:47], v[140:143], v[140:143], 0\n"
      "v_mfma_f32_32x32x16_bf16 a[48:63], v[140:143], v[140:143], 0\n"
      "v_mfma_f32_32x32x16_bf16 a[64:79], v[140:143], v[140:143], 0\n"
      "v_mfma_f32_32x32x16_bf16 a[80:95], v[140:143], v[140:143], 0\n"
      "s_and_b32 s94, s76, 1\n"
      "s_cmp_eq_u32 s94, 1\n"
      "s_cselect_b64 s[96:97], -1, 0\n"
      "v_cndmask_b32 v234, v232, v233, s[96:97]\n"
      "s_lshl_b32 s94, s94, 4\n"
      "s_lshr_b32 s95, s76, 1\n"
      "s_lshl_b32 s95, s95, 15\n"
      "s_or_b32 s94, s94, s95\n"
      "v_xor_b32 v251, s94, v235\n"
      "s_add_u32 s94, s76, 1\n"
      "s_and_b32 s94, s94, 3\n"
      "s_mul_i32 s94, s94, 12288\n"
      "s_add_u32 s94, s94, 65536\n"
      "v_add_u32 v236, s94, v248\n"
      "s_add_u32 s94, s76, 2\n"
      "s_and_b32 s94, s94, 3\n"
      "s_mul_i32 s94, s94, 12288\n"
      "s_add_u32 s94, s94, 69632\n"
      "v_add_u32 v237, s94, v248\n"
      "s_add_u32 s94, s76, 3\n"
      "s_and_b32 s94, s94, 3\n"
      "s_mul_i32 s94, s94, 12288\n"
      "s_add_u32 s94, s94, 73728\n"
      "v_add_u32 v238, s94, v248\n"
      "s_mul_i32 s94, s76, 12288\n"
      "s_or_b32 m0, s62, 32768\n"
      "s_add_u32 s98, s98, 1024\n"
      "buffer_load_dwordx4 v147, s[68:71], s63 offen lds\n"
      "s_add_u32 s94, s94, 65536\n"
      "v_add_u32 v239, s94, v248\n"
      "v_mul_u32_u24 v252, 400, v241\n"
      "v_lshl_add_u32 v252, v242, 2, v252\n"
      "s_lshl_b32 s95, s81, 4\n"
      "s_sub_u32 s96, s99, s81\n"
      "s_lshl_b32 s96, s96, 4\n"
      "s_add_u32 s94, s76, 0\n"
      "s_and_b32 s94, s94, 3\n"
      "s_mul_i32 s94, s94, 6400\n"
      "s_add_u32 s94, s94, 65536\n"
      "s_add_u32 s94, s94, s95\n"
      "v_add_u32 v228, s94, v252\n"
      "v_add_u32 v136, s96, v228\n"
      "s_add_u32 s94, s76, 1\n"
      "s_and_b32 s94, s94, 3\n"
      "s_mul_i32 s94, s94, 6400\n"
      "s_add_u32 s94, s94, 65536\n"
      "s_add_u32 s94, s94, s95\n"
      "v_add_u32 v229, s94, v252\n"
      "v_add_u32 v137, s96, v229\n"
      "s_add_u32 s94, s76, 2\n"
      "s_and_b32 s94, s94, 3\n"
      "s_mul_i32 s94, s94, 6400\n"
      "s_add_u32 s94, s94, 65536\n"
      "s_add_u32 s94, s94, s95\n"
      "v_add_u32 v230, s94, v252\n"
      "v_add_u32 v138, s96, v230\n"
      "s_add_u32 s94, s76, 3\n"
      "s_and_b32 s94, s94, 3\n"
      "s_mul_i32 s94, s94, 6400\n"
      "s_add_u32 s94, s94, 65536\n"
      "s_add_u32 s94, s94, s95\n"
      "v_add_u32 v231, s94, v252\n"
      "v_add_u32 v139, s96, v231\n"
      "s_waitcnt vmcnt(9)\n"
      "v_lshlrev_b32 v252, 1, v240\n"
      "s_add_u32 s93, s93, 116736\n"
      "v_add_u32 v252, s93, v252\n"
      "s_mov_b64 exec, 0xffff\n"
      "ds_write_b16 v252, v146 offset:960\n"
      "s_mov_b64 exec, -1\n"
      "s_lshl_b32 s94, s85, 7\n"
      "s_add_u32 s94, s94, s93\n"
      "v_lshlrev_b32 v253, 6, v250\n"
      "v_add_u32 v253, s94, v253\n"
      "ds_read_b128 v[80:83], v253 offset:0\n"
      "ds_read_b128 v[84:87], v253 offset:16\n"
      "ds_read_b128 v[88:91], v253 offset:32\n"
      "ds_read_b128 v[92:95], v253 offset:48\n"
      "ds_read_b128 v[96:99], v253 offset:256\n"
      "ds_read_b128 v[100:103], v253 offset:272\n"
      "ds_read_b128 v[104:107], v253 offset:288\n"
      "ds_read_b128 v[108:111], v253 offset:304\n"
      "v_mul_u32_u24 v252, 26, v241\n"
      "v_lshl_add_u32 v252, v242, 3, v252\n"
      "v_add_u32 v252, s93, v252\n"
      "ds_read_u16 v112, v252 offset:512\n"
      "ds_read_u16 v120, v252 offset:514\n"
      "ds_read_u16 v113, v252 offset:516\n"
      "ds_read_u16 v121, v252 offset:518\n"
      "v_lshlrev_b32 v255, 3, v242\n"
      "v_add_u32 v255, s93, v255\n"
      "ds_read_b64 v[116:117], v255 offset:960\n"
      "v_mov_b32 v244, 0\n"
      "s_mov_b64 exec, 0x3ffff\n"
      "v_lshlrev_b32_e64 v244, v245, 1\n"
      "s_mov_b64 exec, -1\n"
      "v_mov_b32 v114, 0\n"
      "v_mov_b32 v115, 0\n"
      "v_or_b32_dpp v244, v244, v244 row_ror:1 row_mask:0xf bank_mask:0xf\n"
      "v_mov_b32 v118, 0\n"
      "v_mov_b32 v119, 0\n"
      "v_or_b32_dpp v244, v244, v244 row_ror:2 row_mask:0xf bank_mask:0xf\n"
      "v_mov_b32 v122, 0x01000c0c\n"
      "v_mov_b32 v123, s77\n"
      "v_or_b32_dpp v244, v244, v244 row_ror:4 row_mask:0xf bank_mask:0xf\n"
      "s_mov_b32 exec_lo, 0\n"
      "v_mov_b32 v122, 0x03020c0c\n"
      "v_mov_b32 v123, s78\n"
      "s_mov_b64 exec, -1\n"
      "v_or_b32_dpp v244, v244, v244 row_ror:8 row_mask:0xf bank_mask:0xf\n"
      "s_mov_b32 vcc_lo, 0x00ff00ff\n"
      "s_mov_b32 vcc_hi, 0x00ff00ff\n"
      "v_readlane_b32 s94, v244, 0\n"
      "v_readlane_b32 s95, v244, 16\n"
      "s_waitcnt lgkmcnt(0)\n"
      "s_or_b32 s94, s94, s95\n"
      "s_ff0_i32_b32 s88, s94\n"
      "s_mov_b32 exec_lo, 0\n"
      "s_mov_b32 exec_hi, 0xffff0000\n"
      "v_mov_b32 v120, 0x3f80\n"
      "v_mov_b32 v113, 0x3f80\n"
      "v_mov_b32 v121, 0\n"
      "s_mov_b64 exec, -1\n"
      "v_mov_b32 v245, s88\n"
      "v_lshl_or_b32 v112, v120, 16, v112\n"
      "v_lshl_or_b32 v113, v121, 16, v113\n"
      "v_perm_b32 v148, v80, v80, v122\n"
      "v_perm_b32 v164, v80, v80, v122\n"
      "v_perm_b32 v152, v81, v81, v122\n"
      "v_perm_b32 v168, v81, v81, v122\n"
      "v_perm_b32 v156, v82, v82, v122\n"
      "v_perm_b32 v172, v82, v82, v122\n"
      "v_perm_b32 v160, v83, v83, v122\n"
      "v_perm_b32 v176, v83, v83, v122\n"
      "v_perm_b32 v149, v84, v84, v122\n"
      "v_perm_b32 v165, v84, v84, v122\n"
      "v_perm_b32 v153, v85, v85, v122\n"
      "v_perm_b32 v169, v85, v85, v122\n"
      "v_perm_b32 v157, v86, v86, v122\n"
      "v_perm_b32 v173, v86, v86, v122\n"
      "v_perm_b32 v161, v87, v87, v122\n"
      "v_perm_b32 v177, v87, v87, v122\n"
      "v_perm_b32 v150, v88, v88, v122\n"
      "v_perm_b32 v166, v88, v88, v122\n"
      "v_perm_b32 v154, v89, v89, v122\n"
      "v_perm_b32 v170, v89, v89, v122\n"
      "v_perm_b32 v158, v90, v90, v122\n"
      "v_perm_b32 v174, v90, v90, v122\n"
      "v_perm_b32 v162, v91, v91, v122\n"
      "v_perm_b32 v178, v91, v91, v122\n"
      "v_perm_b32 v151, v92, v92, v122\n"
      "v_perm_b32 v167, v92, v92, v122\n"
      "v_perm_b32 v155, v93, v93, v122\n"
      "v_perm_b32 v171, v93, v93, v122\n"
      "v_perm_b32 v159, v94, v94, v122\n"
      "v_perm_b32 v175, v94, v94, v122\n"
      "v_perm_b32 v163, v95, v95, v122\n"
      "v_perm_b32 v179, v95, v95, v122\n"
      "v_perm_b32 v180, v97, v96, v123\n"
      "v_perm_b32 v181, v99, v98, v123\n"
      "v_perm_b32 v182, v101, v100, v123\n"
      "v_perm_b32 v183, v103, v102, v123\n"
      "v_perm_b32 v184, v105, v104, v123\n"
      "v_perm_b32 v185, v107, v106, v123\n"
      "v_perm_b32 v186, v109, v108, v123\n"
      "v_perm_b32 v187, v111, v110, v123\n"
      "v_mfma_f32_16x16x32_bf16 v[188:191], v[116:119], v[112:115], 0\n"
      "s_add_u32 m0, s82, 25600\n"
      "s_nop 0\n"
      "buffer_load_dwordx4 v248, s[72:75], s89 offen offset:0 lds\n"
      "buffer_load_dwordx4 v248, s[72:75], s89 offen offset:1024 lds\n"
      "buffer_load_dwordx4 v248, s[72:75], s89 offen offset:2048 lds\n"
      "buffer_load_dwordx4 v248, s[72:75], s89 offen offset:3072 lds\n"
      "s_add_u32 m0, s82, 29696\n"
      "s_add_u32 s89, s89, 4096\n"
      "buffer_load_dwordx4 v248, s[72:75], s89 offen offset:0 lds\n"
      "buffer_load_dwordx4 v248, s[72:75], s89 offen offset:1024 lds\n"
      "s_cmp_lg_u32 s76, 3\n"
      "s_cbranch_scc1 Lw3b%=\n"
      "buffer_load_dwordx4 v248, s[72:75], s89 offen offset:2048 lds\n"
      "Lw3b%=:\n"
      "s_and_b32 s98, s98, 0x1fff\n"
      "s_or_b32 s62, s92, s98\n"
      "s_mov_b32 m0, s62\n"
      "s_lshl_b32 s63, s62, 1\n"
      "buffer_load_dwordx4 v249, s[68:71], s63 offen lds\n"
      "s_or_b32 m0, s62, 32768\n"
      "s_add_u32 s98, s98, 1024\n"
      "buffer_load_dwordx4 v147, s[68:71], s63 offen lds\n"
      "s_and_b32 s98, s98, 0x1fff\n"
      "s_or_b32 s62, s92, s98\n"
      "s_mov_b32 m0, s62\n"
      "s_lshl_b32 s63, s62, 1\n"
      "buffer_load_dwordx4 v249, s[68:71], s63 offen lds\n"
      "s_or_b32 m0, s62, 32768\n"
      "s_add_u32 s98, s98, 1024\n"
      "buffer_load_dwordx4 v147, s[68:71], s63 offen lds\n"
      "s_and_b32 s98, s98, 0x1fff\n"
      "s_or_b32 s62, s92, s98\n"
      "s_mov_b32 m0, s62\n"
      "s_lshl_b32 s63, s62, 1\n"
      "buffer_load_dwordx4 v249, s[68:71], s63 offen lds\n"
      "s_or_b32 m0, s62, 32768\n"
      "s_add_u32 s98, s98, 1024\n"
      "buffer_load_dwordx4 v147, s[68:71], s63 offen lds\n"
      "s_and_b32 s98, s98, 0x1fff\n"
      "s_or_b32 s62, s92, s98\n"
      "s_mov_b32 m0, s62\n"
      "s_lshl_b32 s63, s62, 1\n"
      "buffer_load_dwordx4 v249, s[68:71], s63 offen lds\n"
      "s_or_b32 m0, s62, 32768\n"
      "s_add_u32 s98, s98, 1024\n"
      "buffer_load_dwordx4 v147, s[68:71], s63 offen lds\n"
      "s_and_b32 s98, s98, 0x1fff\n"
      "s_or_b32 s62, s92, s98\n"
      "s_mov_b32 m0, s62\n"
      "s_lshl_b32 s63, s62, 1\n"
      "buffer_load_dwordx4 v249, s[68:71], s63 offen lds\n"
      "s_or_b32 m0, s62, 32768\n"
      "s_add_u32 s98, s98, 1024\n"
      "buffer_load_dwordx4 v147, s[68:71], s63 offen lds\n"
      "s_waitcnt vmcnt(10)\n"
      "s_barrier\n"
      "ds_read2st64_b32 v[220:221], v228 offset0:0 offset1:100\n"
      "ds_read2st64_b32 v[222:223], v229 offset0:0 offset1:100\n"
      "ds_read2st64_b32 v[224:225], v230 offset0:0 offset1:100\n"
      "ds_read2st64_b32 v[226:227], v231 offset0:0 offset1:100\n"
      "ds_read2st64_b32 v[124:125], v136 offset0:0 offset1:100\n"
      "ds_read2st64_b32 v[126:127], v137 offset0:0 offset1:100\n"
      "ds_read2st64_b32 v[128:129], v138 offset0:0 offset1:100\n"
      "ds_read2st64_b32 v[130:131], v139 offset0:0 offset1:100\n"
      "s_and_b32 s98, s98, 0x1fff\n"
      "s_or_b32 s62, s92, s98\n"
      "s_mov_b32 m0, s62\n"
      "s_lshl_b32 s63, s62, 1\n"
      "buffer_load_dwordx4 v249, s[68:71], s63 offen lds\n"
      "s_or_b32 m0, s62, 32768\n"
      "s_add_u32 s98, s98, 1024\n"
      "buffer_load_dwordx4 v147, s[68:71], s63 offen lds\n"
      "s_and_b32 s98, s98, 0x1fff\n"
      "s_or_b32 s62, s92, s98\n"
      "s_mov_b32 m0, s62\n"
      "s_lshl_b32 s63, s62, 1\n"
      "buffer_load_dwordx4 v249, s[68:71], s63 offen lds\n"
      "s_or_b32 m0, s62, 32768\n"
      "s_add_u32 s98, s98, 1024\n"
      "buffer_load_dwordx4 v147, s[68:71], s63 offen lds\n"
      "buffer_load_dwordx4 v[0:3], v232, s[64:67], s79 offen offset:0\n"
      "s_waitcnt lgkmcnt(0)\n"
      "v_lshl_add_u32 v196, v220, 5, v235\n"
      "buffer_load_dwordx4 v[4:7], v232, s[64:67], s79 offen offset:256\n"
      "v_lshl_add_u32 v197, v222, 5, v235\n"
      "v_lshl_add_u32 v198, v224, 5, v235\n"
      "buffer_load_dwordx4 v[8:11], v232, s[64:67], s79 offen offset:512\n"
      "v_lshl_add_u32 v199, v226, 5, v235\n"
      "v_lshl_add_u32 v200, v221, 5, v235\n"
      "buffer_load_dwordx4 v[12:15], v232, s[64:67], s79 offen offset:768\n"
      "v_lshl_add_u32 v201, v223, 5, v235\n"
      "v_lshl_add_u32 v202, v225, 5, v235\n"
      "buffer_load_dwordx4 v[16:19], v233, s[64:67], s79 offen offset:2048\n"
      "v_lshl_add_u32 v203, v227, 5, v235\n"
      "v_xor_b32 v204, 16, v196\n"
      "buffer_load_dwordx4 v[20:23], v233, s[64:67], s79 offen offset:2304\n"
      "v_xor_b32 v205, 16, v197\n"
      "v_xor_b32 v206, 16, v198\n"
      "buffer_load_dwordx4 v[24:27], v233, s[64:67], s79 offen offset:2560\n"
      "v_xor_b32 v207, 16, v199\n"
      "v_xor_b32 v208, 16, v200\n"
      "buffer_load_dwordx4 v[28:31], v233, s[64:67], s79 offen offset:2816\n"
      "v_xor_b32 v209, 16, v201\n"
      "v_xor_b32 v210, 16, v202\n"
      "v_xor_b32 v211, 16, v203\n"
      "s_waitcnt vmcnt(8)\n"
      "s_barrier\n"
      "ds_read_b128 a[96:99], v196\n"
      "ds_read_b128 a[100:103], v197\n"
      "ds_read_b128 a[104:107], v198\n"
      "ds_read_b128 a[108:111], v199\n"
      "ds_read_b128 a[112:115], v200\n"
      "ds_read_b128 a[116:119], v201\n"
      "v_lshl_add_u32 v212, v124, 5, v251\n"
      "ds_read_b128 a[120:123], v202\n"
      "v_lshl_add_u32 v213, v126, 5, v251\n"
      "ds_read_b128 a[124:127], v203\n"
      "v_lshl_add_u32 v214, v128, 5, v251\n"
      "buffer_load_dwordx4 v[32:35], v232, s[64:67], s80 offen offset:0\n"
      "v_lshl_add_u32 v215, v130, 5, v251\n"
      "buffer_load_dwordx4 v[36:39], v232, s[64:67], s80 offen offset:256\n"
      "v_lshl_add_u32 v216, v125, 5, v251\n"
      "buffer_load_dwordx4 v[40:43], v232, s[64:67], s80 offen offset:512\n"
      "v_lshl_add_u32 v217, v127, 5, v251\n"
      "buffer_load_dwordx4 v[44:47], v232, s[64:67], s80 offen offset:768\n"
      "v_lshl_add_u32 v218, v129, 5, v251\n"
      "buffer_load_dwordx4 v[48:51], v233, s[64:67], s80 offen offset:2048\n"
      "v_lshl_add_u32 v219, v131, 5, v251\n"
      "buffer_load_dwordx4 v[52:55], v233, s[64:67], s80 offen offset:2304\n"
      "s_mov_b32 s82, 0\n"
      "s_mov_b64 exec, 1\n"
      "global_store_dwordx2 v243, v[244:245], s[36:37] offset:256 sc1\n"
      "s_mov_b64 exec, -1\n"
      "s_waitcnt vmcnt(11)\n"
      "v_perm_b32 v66, v4, v0, s78\n"
      "v_perm_b32 v67, v12, v8, s78\n"
      "v_perm_b32 v144, v4, v0, s77\n"
      "v_perm_b32 v145, v12, v8, s77\n"
      "v_cndmask_b32_dpp v64, v66, v144, vcc row_ror:8 row_mask:0xf bank_mask:0xf\n"
      "v_cndmask_b32_dpp v65, v67, v145, vcc row_ror:8 row_mask:0xf bank_mask:0xf\n"
      "v_mov_b32_dpp v66, v144 row_ror:8 row_mask:0xf bank_mask:0x3\n"
      "v_mov_b32_dpp v67, v145 row_ror:8 row_mask:0xf bank_mask:0x3\n"
      "s_waitcnt lgkmcnt(0)\n"
      "s_nop 0\n"
      "Lloop%=:\n"
      "v_mfma_f32_16x16x32_bf16 v[148:151], v[64:67], a[96:99], v[148:151]\n"
      "v_perm_b32 v70, v5, v1, s78\n"
      "v_perm_b32 v71, v13, v9, s78\n"
      "v_mfma_f32_16x16x32_bf16 a[0:3], v[64:67], a[100:103], a[0:3]\n"
      "v_perm_b32 v144, v5, v1, s77\n"
      "v_perm_b32 v145, v13, v9, s77\n"
      "v_mfma_f32_16x16x32_bf16 a[16:19], v[64:67], a[104:107], a[16:19]\n"
      "ds_read_b128 a[128:131], v204 offset:0\n"
      "s_add_u32 s81, s81, 1\n"
      "v_mfma_f32_16x16x32_bf16 a[32:35], v[64:67], a[108:111], a[32:35]\n"
      "v_cndmask_b32_dpp v68, v70, v144, vcc row_ror:8 row_mask:0xf bank_mask:0xf\n"
      "v_cndmask_b32_dpp v69, v71, v145, vcc row_ror:8 row_mask:0xf bank_mask:0xf\n"
      "v_mfma_f32_16x16x32_bf16 v[164:167], v[64:67], a[112:115], v[164:167]\n"
      "v_mov_b32_dpp v70, v144 row_ror:8 row_mask:0xf bank_mask:0x3\n"
      "v_mov_b32_dpp v71, v145 row_ror:8 row_mask:0xf bank_mask:0x3\n"
      "v_mfma_f32_16x16x32_bf16 a[48:51], v[64:67], a[116:119], a[48:51]\n"
      "buffer_load_dwordx4 v[56:59], v233, s[64:67], s80 offen offset:2560\n"
      "s_cmp_eq_u32 s81, 25\n"
      "v_mfma_f32_16x16x32_bf16 a[64:67], v[64:67], a[120:123], a[64:67]\n"
      "ds_read_b128 a[132:135], v205 offset:0\n"
      "s_cselect_b32 s81, 0, s81\n"
      "v_mfma_f32_16x16x32_bf16 a[80:83], v[64:67], a[124:127], a[80:83]\n"
      "ds_read_b128 a[136:139], v206 offset:0\n"
      "s_cselect_b32 s89, -384, 16\n"
      "v_mfma_f32_16x16x32_bf16 v[152:155], v[68:71], a[96:99], v[152:155]\n"
      "v_perm_b32 v66, v6, v2, s78\n"
      "v_perm_b32 v67, v14, v10, s78\n"
      "v_mfma_f32_16x16x32_bf16 a[4:7], v[68:71], a[100:103], a[4:7]\n"
      "v_perm_b32 v144, v6, v2, s77\n"
      "v_perm_b32 v145, v14, v10, s77\n"
      "v_mfma_f32_16x16x32_bf16 a[20:23], v[68:71], a[104:107], a[20:23]\n"
      "ds_read_b128 a[140:143], v207 offset:0\n"
      "s_lshl_b32 s79, s81, 15\n"
      "v_mfma_f32_16x16x32_bf16 a[36:39], v[68:71], a[108:111], a[36:39]\n"
      "v_cndmask_b32_dpp v64, v66, v144, vcc row_ror:8 row_mask:0xf bank_mask:0xf\n"
      "v_cndmask_b32_dpp v65, v67, v145, vcc row_ror:8 row_mask:0xf bank_mask:0xf\n"
      "v_mfma_f32_16x16x32_bf16 v[168:171], v[68:71], a[112:115], v[168:171]\n"
      "v_mov_b32_dpp v66, v144 row_ror:8 row_mask:0xf bank_mask:0x3\n"
      "v_mov_b32_dpp v67, v145 row_ror:8 row_mask:0xf bank_mask:0x3\n"
      "v_mfma_f32_16x16x32_bf16 a[52:55], v[68:71], a[116:119], a[52:55]\n"
      "buffer_load_dwordx4 v[60:63], v233, s[64:67], s80 offen offset:2816\n"
      "s_add_u32 s82, s82, 1\n"
      "v_mfma_f32_16x16x32_bf16 a[68:71], v[68:71], a[120:123], a[68:71]\n"
      "ds_read_b128 a[144:147], v208 offset:0\n"
      "v_add_u32 v228, s89, v228\n"
      "v_mfma_f32_16x16x32_bf16 a[84:87], v[68:71], a[124:127], a[84:87]\n"
      "ds_read_b128 a[148:151], v209 offset:0\n"
      "v_add_u32 v229, s89, v229\n"
      "v_mfma_f32_16x16x32_bf16 v[156:159], v[64:67], a[96:99], v[156:159]\n"
      "v_perm_b32 v70, v7, v3, s78\n"
      "v_perm_b32 v71, v15, v11, s78\n"
      "v_mfma_f32_16x16x32_bf16 a[8:11], v[64:67], a[100:103], a[8:11]\n"
      "v_perm_b32 v144, v7, v3, s77\n"
      "v_perm_b32 v145, v15, v11, s77\n"
      "v_mfma_f32_16x16x32_bf16 a[24:27], v[64:67], a[104:107], a[24:27]\n"
      "ds_read_b128 a[152:155], v210 offset:0\n"
      "v_add_u32 v230, s89, v230\n"
      "v_mfma_f32_16x16x32_bf16 a[40:43], v[64:67], a[108:111], a[40:43]\n"
      "v_cndmask_b32_dpp v68, v70, v144, vcc row_ror:8 row_mask:0xf bank_mask:0xf\n"
      "v_cndmask_b32_dpp v69, v71, v145, vcc row_ror:8 row_mask:0xf bank_mask:0xf\n"
      "v_mfma_f32_16x16x32_bf16 v[172:175], v[64:67], a[112:115], v[172:175]\n"
      "v_mov_b32_dpp v70, v144 row_ror:8 row_mask:0xf bank_mask:0x3\n"
      "v_mov_b32_dpp v71, v145 row_ror:8 row_mask:0xf bank_mask:0x3\n"
      "v_mfma_f32_16x16x32_bf16 a[56:59], v[64:67], a[116:119], a[56:59]\n"
      "buffer_load_dwordx4 v[0:3], v232, s[64:67], s79 offen offset:0\n"
      "v_add_u32 v231, s89, v231\n"
      "v_mfma_f32_16x16x32_bf16 a[72:75], v[64:67], a[120:123], a[72:75]\n"
      "ds_read_b128 a[156:159], v211 offset:0\n"
      "v_mfma_f32_16x16x32_bf16 a[88:91], v[64:67], a[124:127], a[88:91]\n"
      "ds_read2st64_b32 v[220:221], v228 offset0:0 offset1:100\n"
      "v_mfma_f32_16x16x32_bf16 v[160:163], v[68:71], a[96:99], v[160:163]\n"
      "s_waitcnt vmcnt(9)\n"
      "v_perm_b32 v66, v20, v16, s78\n"
      "v_perm_b32 v67, v28, v24, s78\n"
      "v_mfma_f32_16x16x32_bf16 a[12:15], v[68:71], a[100:103], a[12:15]\n"
      "v_perm_b32 v144, v20, v16, s77\n"
      "v_perm_b32 v145, v28, v24, s77\n"
      "v_mfma_f32_16x16x32_bf16 a[28:31], v[68:71], a[104:107], a[28:31]\n"
      "ds_read2st64_b32 v[222:223], v229 offset0:0 offset1:100\n"
      "ds_read2st64_b32 v[224:225], v230 offset0:0 offset1:100\n"
      "v_mfma_f32_16x16x32_bf16 a[44:47], v[68:71], a[108:111], a[44:47]\n"
      "v_cndmask_b32_dpp v64, v66, v144, vcc row_ror:8 row_mask:0xf bank_mask:0xf\n"
      "v_cndmask_b32_dpp v65, v67, v145, vcc row_ror:8 row_mask:0xf bank_mask:0xf\n"
      "v_mfma_f32_16x16x32_bf16 v[176:179], v[68:71], a[112:115], v[176:179]\n"
      "v_mov_b32_dpp v66, v144 row_ror:8 row_mask:0xf bank_mask:0x3\n"
      "v_mov_b32_dpp v67, v145 row_ror:8 row_mask:0xf bank_mask:0x3\n"
      "v_mfma_f32_16x16x32_bf16 a[60:63], v[68:71], a[116:119], a[60:63]\n"
      "buffer_load_dwordx4 v[4:7], v232, s[64:67], s79 offen offset:256\n"
      "ds_read2st64_b32 v[226:227], v231 offset0:0 offset1:100\n"
      "v_mfma_f32_16x16x32_bf16 a[76:79], v[68:71], a[120:123], a[76:79]\n"
      "v_mfma_f32_16x16x32_bf16 a[92:95], v[68:71], a[124:127], a[92:95]\n"
      "s_waitcnt lgkmcnt(4)\n"
      "v_mfma_f32_16x16x32_bf16 v[148:151], v[64:67], a[128:131], v[148:151]\n"
      "v_perm_b32 v70, v21, v17, s78\n"
      "v_perm_b32 v71, v29, v25, s78\n"
      "v_mfma_f32_16x16x32_bf16 a[0:3], v[64:67], a[132:135], a[0:3]\n"
      "v_perm_b32 v144, v21, v17, s77\n"
      "v_perm_b32 v145, v29, v25, s77\n"
      "v_mfma_f32_16x16x32_bf16 a[16:19], v[64:67], a[136:139], a[16:19]\n"
      "ds_read_b128 a[96:99], v196 offset:32768\n"
      "s_add_u32 s80, s79, 4096\n"
      "v_mfma_f32_16x16x32_bf16 a[32:35], v[64:67], a[140:143], a[32:35]\n"
      "v_cndmask_b32_dpp v68, v70, v144, vcc row_ror:8 row_mask:0xf bank_mask:0xf\n"
      "v_cndmask_b32_dpp v69, v71, v145, vcc row_ror:8 row_mask:0xf bank_mask:0xf\n"
      "v_mfma_f32_16x16x32_bf16 v[164:167], v[64:67], a[144:147], v[164:167]\n"
      "v_mov_b32_dpp v70, v144 row_ror:8 row_mask:0xf bank_mask:0x3\n"
      "v_mov_b32_dpp v71, v145 row_ror:8 row_mask:0xf bank_mask:0x3\n"
      "v_mfma_f32_16x16x32_bf16 a[48:51], v[64:67], a[148:151], a[48:51]\n"
      "buffer_load_dwordx4 v[8:11], v232, s[64:67], s79 offen offset:512\n"
      "s_cmp_lt_u32 s82, 5\n"
      "v_mfma_f32_16x16x32_bf16 a[64:67], v[64:67], a[152:155], a[64:67]\n"
      "ds_read_b128 a[100:103], v197 offset:32768\n"
      "v_mfma_f32_16x16x32_bf16 a[80:83], v[64:67], a[156:159], a[80:83]\n"
      "ds_read_b128 a[104:107], v198 offset:32768\n"
      "v_mfma_f32_16x16x32_bf16 v[152:155], v[68:71], a[128:131], v[152:155]\n"
      "v_perm_b32 v66, v22, v18, s78\n"
      "v_perm_b32 v67, v30, v26, s78\n"
      "v_mfma_f32_16x16x32_bf16 a[4:7], v[68:71], a[132:135], a[4:7]\n"
      "v_perm_b32 v144, v22, v18, s77\n"
      "v_perm_b32 v145, v30, v26, s77\n"
      "v_mfma_f32_16x16x32_bf16 a[20:23], v[68:71], a[136:139], a[20:23]\n"
      "ds_read_b128 a[108:111], v199 offset:32768\n"
      "v_mfma_f32_16x16x32_bf16 a[36:39], v[68:71], a[140:143], a[36:39]\n"
      "v_cndmask_b32_dpp v64, v66, v144, vcc row_ror:8 row_mask:0xf bank_mask:0xf\n"
      "v_cndmask_b32_dpp v65, v67, v145, vcc row_ror:8 row_mask:0xf bank_mask:0xf\n"
      "v_mfma_f32_16x16x32_bf16 v[168:171], v[68:71], a[144:147], v[168:171]\n"
      "v_mov_b32_dpp v66, v144 row_ror:8 row_mask:0xf bank_mask:0x3\n"
      "v_mov_b32_dpp v67, v145 row_ror:8 row_mask:0xf bank_mask:0x3\n"
      "v_mfma_f32_16x16x32_bf16 a[52:55], v[68:71], a[148:151], a[52:55]\n"
      "buffer_load_dwordx4 v[12:15], v232, s[64:67], s79 offen offset:768\n"
      "v_mfma_f32_16x16x32_bf16 a[68:71], v[68:71], a[152:155], a[68:71]\n"
      "ds_read_b128 a[112:115], v200 offset:32768\n"
      "v_mfma_f32_16x16x32_bf16 a[84:87], v[68:71], a[156:159], a[84:87]\n"
      "ds_read_b128 a[116:119], v201 offset:32768\n"
      "v_mfma_f32_16x16x32_bf16 v[156:159], v[64:67], a[128:131], v[156:159]\n"
      "v_perm_b32 v70, v23, v19, s78\n"
      "v_perm_b32 v71, v31, v27, s78\n"
      "v_mfma_f32_16x16x32_bf16 a[8:11], v[64:67], a[132:135], a[8:11]\n"
      "v_perm_b32 v144, v23, v19, s77\n"
      "v_perm_b32 v145, v31, v27, s77\n"
      "v_mfma_f32_16x16x32_bf16 a[24:27], v[64:67], a[136:139], a[24:27]\n"
      "ds_read_b128 a[120:123], v202 offset:32768\n"
      "v_mfma_f32_16x16x32_bf16 a[40:43], v[64:67], a[140:143], a[40:43]\n"
      "v_cndmask_b32_dpp v68, v70, v144, vcc row_ror:8 row_mask:0xf bank_mask:0xf\n"
      "v_cndmask_b32_dpp v69, v71, v145, vcc row_ror:8 row_mask:0xf bank_mask:0xf\n"
      "v_mfma_f32_16x16x32_bf16 v[172:175], v[64:67], a[144:147], v[172:175]\n"
      "v_mov_b32_dpp v70, v144 row_ror:8 row_mask:0xf bank_mask:0x3\n"
      "v_mov_b32_dpp v71, v145 row_ror:8 row_mask:0xf bank_mask:0x3\n"
      "v_mfma_f32_16x16x32_bf16 a[56:59], v[64:67], a[148:151], a[56:59]\n"
      "buffer_load_dwordx4 v[16:19], v233, s[64:67], s79 offen offset:2048\n"
      "v_mfma_f32_16x16x32_bf16 a[72:75], v[64:67], a[152:155], a[72:75]\n"
      "ds_read_b128 a[124:127], v203 offset:32768\n"
      "v_mfma_f32_16x16x32_bf16 a[88:91], v[64:67], a[156:159], a[88:91]\n"
      "v_mfma_f32_16x16x32_bf16 v[160:163], v[68:71], a[128:131], v[160:163]\n"
      "s_waitcnt vmcnt(9)\n"
      "v_perm_b32 v66, v36, v32, s78\n"
      "v_perm_b32 v67, v44, v40, s78\n"
      "v_mfma_f32_16x16x32_bf16 a[12:15], v[68:71], a[132:135], a[12:15]\n"
      "v_perm_b32 v144, v36, v32, s77\n"
      "v_perm_b32 v145, v44, v40, s77\n"
      "v_mfma_f32_16x16x32_bf16 a[28:31], v[68:71], a[136:139], a[28:31]\n"
      "v_mfma_f32_16x16x32_bf16 a[44:47], v[68:71], a[140:143], a[44:47]\n"
      "v_cndmask_b32_dpp v64, v66, v144, vcc row_ror:8 row_mask:0xf bank_mask:0xf\n"
      "v_cndmask_b32_dpp v65, v67, v145, vcc row_ror:8 row_mask:0xf bank_mask:0xf\n"
      "v_mfma_f32_16x16x32_bf16 v[176:179], v[68:71], a[144:147], v[176:179]\n"
      "v_mov_b32_dpp v66, v144 row_ror:8 row_mask:0xf bank_mask:0x3\n"
      "v_mov_b32_dpp v67, v145 row_ror:8 row_mask:0xf bank_mask:0x3\n"
      "v_mfma_f32_16x16x32_bf16 a[60:63], v[68:71], a[148:151], a[60:63]\n"
      "buffer_load_dwordx4 v[20:23], v233, s[64:67], s79 offen offset:2304\n"
      "v_mfma_f32_16x16x32_bf16 a[76:79], v[68:71], a[152:155], a[76:79]\n"
      "v_mfma_f32_16x16x32_bf16 a[92:95], v[68:71], a[156:159], a[92:95]\n"
      "s_waitcnt lgkmcnt(0)\n"
      "v_mfma_f32_16x16x32_bf16 v[148:151], v[64:67], a[96:99], v[148:151]\n"
      "v_perm_b32 v70, v37, v33, s78\n"
      "v_perm_b32 v71, v45, v41, s78\n"
      "v_mfma_f32_16x16x32_bf16 a[0:3], v[64:67], a[100:103], a[0:3]\n"
      "v_perm_b32 v144, v37, v33, s77\n"
      "v_perm_b32 v145, v45, v41, s77\n"
      "v_mfma_f32_16x16x32_bf16 a[16:19], v[64:67], a[104:107], a[16:19]\n"
      "ds_read_b128 a[128:131], v204 offset:32768\n"
      "v_lshl_add_u32 v196, v220, 5, v235\n"
      "v_mfma_f32_16x16x32_bf16 a[32:35], v[64:67], a[108:111], a[32:35]\n"
      "v_cndmask_b32_dpp v68, v70, v144, vcc row_ror:8 row_mask:0xf bank_mask:0xf\n"
      "v_cndmask_b32_dpp v69, v71, v145, vcc row_ror:8 row_mask:0xf bank_mask:0xf\n"
      "v_mfma_f32_16x16x32_bf16 v[164:167], v[64:67], a[112:115], v[164:167]\n"
      "v_mov_b32_dpp v70, v144 row_ror:8 row_mask:0xf bank_mask:0x3\n"
      "v_mov_b32_dpp v71, v145 row_ror:8 row_mask:0xf bank_mask:0x3\n"
      "v_mfma_f32_16x16x32_bf16 a[48:51], v[64:67], a[116:119], a[48:51]\n"
      "buffer_load_dwordx4 v[24:27], v233, s[64:67], s79 offen offset:2560\n"
      "v_lshl_add_u32 v197, v222, 5, v235\n"
      "v_mfma_f32_16x16x32_bf16 a[64:67], v[64:67], a[120:123], a[64:67]\n"
      "ds_read_b128 a[132:135], v205 offset:32768\n"
      "v_lshl_add_u32 v198, v224, 5, v235\n"
      "v_mfma_f32_16x16x32_bf16 a[80:83], v[64:67], a[124:127], a[80:83]\n"
      "ds_read_b128 a[136:139], v206 offset:32768\n"
      "v_lshl_add_u32 v199, v226, 5, v235\n"
      "v_mfma_f32_16x16x32_bf16 v[152:155], v[68:71], a[96:99], v[152:155]\n"
      "v_perm_b32 v66, v38, v34, s78\n"
      "v_perm_b32 v67, v46, v42, s78\n"
      "v_mfma_f32_16x16x32_bf16 a[4:7], v[68:71], a[100:103], a[4:7]\n"
      "v_perm_b32 v144, v38, v34, s77\n"
      "v_perm_b32 v145, v46, v42, s77\n"
      "v_mfma_f32_16x16x32_bf16 a[20:23], v[68:71], a[104:107], a[20:23]\n"
      "ds_read_b128 a[140:143], v207 offset:32768\n"
      "v_lshl_add_u32 v200, v221, 5, v235\n"
      "v_mfma_f32_16x16x32_bf16 a[36:39], v[68:71], a[108:111], a[36:39]\n"
      "v_cndmask_b32_dpp v64, v66, v144, vcc row_ror:8 row_mask:0xf bank_mask:0xf\n"
      "v_cndmask_b32_dpp v65, v67, v145, vcc row_ror:8 row_mask:0xf bank_mask:0xf\n"
      "v_mfma_f32_16x16x32_bf16 v[168:171], v[68:71], a[112:115], v[168:171]\n"
      "v_mov_b32_dpp v66, v144 row_ror:8 row_mask:0xf bank_mask:0x3\n"
      "v_mov_b32_dpp v67, v145 row_ror:8 row_mask:0xf bank_mask:0x3\n"
      "v_mfma_f32_16x16x32_bf16 a[52:55], v[68:71], a[116:119], a[52:55]\n"
      "buffer_load_dwordx4 v[28:31], v233, s[64:67], s79 offen offset:2816\n"
      "v_lshl_add_u32 v201, v223, 5, v235\n"
      "v_mfma_f32_16x16x32_bf16 a[68:71], v[68:71], a[120:123], a[68:71]\n"
      "ds_read_b128 a[144:147], v208 offset:32768\n"
      "v_lshl_add_u32 v202, v225, 5, v235\n"
      "v_mfma_f32_16x16x32_bf16 a[84:87], v[68:71], a[124:127], a[84:87]\n"
      "ds_read_b128 a[148:151], v209 offset:32768\n"
      "v_lshl_add_u32 v203, v227, 5, v235\n"
      "v_mfma_f32_16x16x32_bf16 v[156:159], v[64:67], a[96:99], v[156:159]\n"
      "v_perm_b32 v70, v39, v35, s78\n"
      "v_perm_b32 v71, v47, v43, s78\n"
      "v_mfma_f32_16x16x32_bf16 a[8:11], v[64:67], a[100:103], a[8:11]\n"
      "v_perm_b32 v144, v39, v35, s77\n"
      "v_perm_b32 v145, v47, v43, s77\n"
      "v_mfma_f32_16x16x32_bf16 a[24:27], v[64:67], a[104:107], a[24:27]\n"
      "ds_read_b128 a[152:155], v210 offset:32768\n"
      "v_mfma_f32_16x16x32_bf16 a[40:43], v[64:67], a[108:111], a[40:43]\n"
      "v_cndmask_b32_dpp v68, v70, v144, vcc row_ror:8 row_mask:0xf bank_mask:0xf\n"
      "v_cndmask_b32_dpp v69, v71, v145, vcc row_ror:8 row_mask:0xf bank_mask:0xf\n"
      "v_mfma_f32_16x16x32_bf16 v[172:175], v[64:67], a[112:115], v[172:175]\n"
      "v_mov_b32_dpp v70, v144 row_ror:8 row_mask:0xf bank_mask:0x3\n"
      "v_mov_b32_dpp v71, v145 row_ror:8 row_mask:0xf bank_mask:0x3\n"
      "v_mfma_f32_16x16x32_bf16 a[56:59], v[64:67], a[116:119], a[56:59]\n"
      "buffer_load_dwordx4 v[32:35], v232, s[64:67], s80 offen offset:0\n"
      "v_mfma_f32_16x16x32_bf16 a[72:75], v[64:67], a[120:123], a[72:75]\n"
      "ds_read_b128 a[156:159], v211 offset:32768\n"
      "v_mfma_f32_16x16x32_bf16 a[88:91], v[64:67], a[124:127], a[88:91]\n"
      "v_mfma_f32_16x16x32_bf16 v[160:163], v[68:71], a[96:99], v[160:163]\n"
      "s_waitcnt vmcnt(9)\n"
      "v_perm_b32 v66, v52, v48, s78\n"
      "v_perm_b32 v67, v60, v56, s78\n"
      "v_mfma_f32_16x16x32_bf16 a[12:15], v[68:71], a[100:103], a[12:15]\n"
      "v_perm_b32 v144, v52, v48, s77\n"
      "v_perm_b32 v145, v60, v56, s77\n"
      "v_mfma_f32_16x16x32_bf16 a[28:31], v[68:71], a[104:107], a[28:31]\n"
      "v_mfma_f32_16x16x32_bf16 a[44:47], v[68:71], a[108:111], a[44:47]\n"
      "v_cndmask_b32_dpp v64, v66, v144, vcc row_ror:8 row_mask:0xf bank_mask:0xf\n"
      "v_cndmask_b32_dpp v65, v67, v145, vcc row_ror:8 row_mask:0xf bank_mask:0xf\n"
      "v_mfma_f32_16x16x32_bf16 v[176:179], v[68:71], a[112:115], v[176:179]\n"
      "v_mov_b32_dpp v66, v144 row_ror:8 row_mask:0xf bank_mask:0x3\n"
      "v_mov_b32_dpp v67, v145 row_ror:8 row_mask:0xf bank_mask:0x3\n"
      "v_mfma_f32_16x16x32_bf16 a[60:63], v[68:71], a[116:119], a[60:63]\n"
      "buffer_load_dwordx4 v[36:39], v232, s[64:67], s80 offen offset:256\n"
      "v_mfma_f32_16x16x32_bf16 a[76:79], v[68:71], a[120:123], a[76:79]\n"
      "v_mfma_f32_16x16x32_bf16 a[92:95], v[68:71], a[124:127], a[92:95]\n"
      "s_waitcnt lgkmcnt(0)\n"
      "v_mfma_f32_16x16x32_bf16 v[148:151], v[64:67], a[128:131], v[148:151]\n"
      "v_perm_b32 v70, v53, v49, s78\n"
      "v_perm_b32 v71, v61, v57, s78\n"
      "v_mfma_f32_16x16x32_bf16 a[0:3], v[64:67], a[132:135], a[0:3]\n"
      "v_perm_b32 v144, v53, v49, s77\n"
      "v_perm_b32 v145, v61, v57, s77\n"
      "v_mfma_f32_16x16x32_bf16 a[16:19], v[64:67], a[136:139], a[16:19]\n"
      "ds_read_b128 a[96:99], v196 offset:0\n"
      "v_xor_b32 v204, 16, v196\n"
      "v_mfma_f32_16x16x32_bf16 a[32:35], v[64:67], a[140:143], a[32:35]\n"
      "v_cndmask_b32_dpp v68, v70, v144, vcc row_ror:8 row_mask:0xf bank_mask:0xf\n"
      "v_cndmask_b32_dpp v69, v71, v145, vcc row_ror:8 row_mask:0xf bank_mask:0xf\n"
      "v_mfma_f32_16x16x32_bf16 v[164:167], v[64:67], a[144:147], v[164:167]\n"
      "v_mov_b32_dpp v70, v144 row_ror:8 row_mask:0xf bank_mask:0x3\n"
      "v_mov_b32_dpp v71, v145 row_ror:8 row_mask:0xf bank_mask:0x3\n"
      "v_mfma_f32_16x16x32_bf16 a[48:51], v[64:67], a[148:151], a[48:51]\n"
      "buffer_load_dwordx4 v[40:43], v232, s[64:67], s80 offen offset:512\n"
      "v_xor_b32 v205, 16, v197\n"
      "v_mfma_f32_16x16x32_bf16 a[64:67], v[64:67], a[152:155], a[64:67]\n"
      "ds_read_b128 a[100:103], v197 offset:0\n"
      "v_xor_b32 v206, 16, v198\n"
      "v_mfma_f32_16x16x32_bf16 a[80:83], v[64:67], a[156:159], a[80:83]\n"
      "ds_read_b128 a[104:107], v198 offset:0\n"
      "v_xor_b32 v207, 16, v199\n"
      "v_mfma_f32_16x16x32_bf16 v[152:155], v[68:71], a[128:131], v[152:155]\n"
      "v_perm_b32 v66, v54, v50, s78\n"
      "v_perm_b32 v67, v62, v58, s78\n"
      "v_mfma_f32_16x16x32_bf16 a[4:7], v[68:71], a[132:135], a[4:7]\n"
      "v_perm_b32 v144, v54, v50, s77\n"
      "v_perm_b32 v145, v62, v58, s77\n"
      "v_mfma_f32_16x16x32_bf16 a[20:23], v[68:71], a[136:139], a[20:23]\n"
      "ds_read_b128 a[108:111], v199 offset:0\n"
      "v_xor_b32 v208, 16, v200\n"
      "v_mfma_f32_16x16x32_bf16 a[36:39], v[68:71], a[140:143], a[36:39]\n"
      "v_cndmask_b32_dpp v64, v66, v144, vcc row_ror:8 row_mask:0xf bank_mask:0xf\n"
      "v_cndmask_b32_dpp v65, v67, v145, vcc row_ror:8 row_mask:0xf bank_mask:0xf\n"
      "v_mfma_f32_16x16x32_bf16 v[168:171], v[68:71], a[144:147], v[168:171]\n"
      "v_mov_b32_dpp v66, v144 row_ror:8 row_mask:0xf bank_mask:0x3\n"
      "v_mov_b32_dpp v67, v145 row_ror:8 row_mask:0xf bank_mask:0x3\n"
      "v_mfma_f32_16x16x32_bf16 a[52:55], v[68:71], a[148:151], a[52:55]\n"
      "buffer_load_dwordx4 v[44:47], v232, s[64:67], s80 offen offset:768\n"
      "v_xor_b32 v209, 16, v201\n"
      "v_mfma_f32_16x16x32_bf16 a[68:71], v[68:71], a[152:155], a[68:71]\n"
      "ds_read_b128 a[112:115], v200 offset:0\n"
      "v_xor_b32 v210, 16, v202\n"
      "v_mfma_f32_16x16x32_bf16 a[84:87], v[68:71], a[156:159], a[84:87]\n"
      "ds_read_b128 a[116:119], v201 offset:0\n"
      "v_xor_b32 v211, 16, v203\n"
      "v_mfma_f32_16x16x32_bf16 v[156:159], v[64:67], a[128:131], v[156:159]\n"
      "v_perm_b32 v70, v55, v51, s78\n"
      "v_perm_b32 v71, v63, v59, s78\n"
      "v_mfma_f32_16x16x32_bf16 a[8:11], v[64:67], a[132:135], a[8:11]\n"
      "v_perm_b32 v144, v55, v51, s77\n"
      "v_perm_b32 v145, v63, v59, s77\n"
      "v_mfma_f32_16x16x32_bf16 a[24:27], v[64:67], a[136:139], a[24:27]\n"
      "ds_read_b128 a[120:123], v202 offset:0\n"
      "v_mfma_f32_16x16x32_bf16 a[40:43], v[64:67], a[140:143], a[40:43]\n"
      "v_cndmask_b32_dpp v68, v70, v144, vcc row_ror:8 row_mask:0xf bank_mask:0xf\n"
      "v_cndmask_b32_dpp v69, v71, v145, vcc row_ror:8 row_mask:0xf bank_mask:0xf\n"
      "v_mfma_f32_16x16x32_bf16 v[172:175], v[64:67], a[144:147], v[172:175]\n"
      "v_mov_b32_dpp v70, v144 row_ror:8 row_mask:0xf bank_mask:0x3\n"
      "v_mov_b32_dpp v71, v145 row_ror:8 row_mask:0xf bank_mask:0x3\n"
      "v_mfma_f32_16x16x32_bf16 a[56:59], v[64:67], a[148:151], a[56:59]\n"
      "buffer_load_dwordx4 v[48:51], v233, s[64:67], s80 offen offset:2048\n"
      "v_mfma_f32_16x16x32_bf16 a[72:75], v[64:67], a[152:155], a[72:75]\n"
      "ds_read_b128 a[124:127], v203 offset:0\n"
      "v_mfma_f32_16x16x32_bf16 a[88:91], v[64:67], a[156:159], a[88:91]\n"
      "v_mfma_f32_16x16x32_bf16 v[160:163], v[68:71], a[128:131], v[160:163]\n"
      "s_waitcnt vmcnt(9)\n"
      "v_perm_b32 v66, v4, v0, s78\n"
      "v_perm_b32 v67, v12, v8, s78\n"
      "v_mfma_f32_16x16x32_bf16 a[12:15], v[68:71], a[132:135], a[12:15]\n"
      "v_perm_b32 v144, v4, v0, s77\n"
      "v_perm_b32 v145, v12, v8, s77\n"
      "v_mfma_f32_16x16x32_bf16 a[28:31], v[68:71], a[136:139], a[28:31]\n"
      "v_mfma_f32_16x16x32_bf16 a[44:47], v[68:71], a[140:143], a[44:47]\n"
      "v_cndmask_b32_dpp v64, v66, v144, vcc row_ror:8 row_mask:0xf bank_mask:0xf\n"
      "v_cndmask_b32_dpp v65, v67, v145, vcc row_ror:8 row_mask:0xf bank_mask:0xf\n"
      "v_mfma_f32_16x16x32_bf16 v[176:179], v[68:71], a[144:147], v[176:179]\n"
      "v_mov_b32_dpp v66, v144 row_ror:8 row_mask:0xf bank_mask:0x3\n"
      "v_mov_b32_dpp v67, v145 row_ror:8 row_mask:0xf bank_mask:0x3\n"
      "v_mfma_f32_16x16x32_bf16 a[60:63], v[68:71], a[148:151], a[60:63]\n"
      "buffer_load_dwordx4 v[52:55], v233, s[64:67], s80 offen offset:2304\n"
      "v_mfma_f32_16x16x32_bf16 a[76:79], v[68:71], a[152:155], a[76:79]\n"
      "v_mfma_f32_16x16x32_bf16 a[92:95], v[68:71], a[156:159], a[92:95]\n"
      "s_waitcnt lgkmcnt(0)\n"
      "s_cbranch_scc1 Lloop%=\n"
      "s_barrier\n"
      "v_mfma_f32_16x16x32_bf16 a[48:51], v[64:67], a[116:119], a[48:51]\n"
      "ds_read_b128 a[164:167], v209 offset:0\n"
      "v_perm_b32 v70, v5, v1, s78\n"
      "v_perm_b32 v71, v13, v9, s78\n"
      "v_perm_b32 v144, v5, v1, s77\n"
      "v_mfma_f32_16x16x32_bf16 a[64:67], v[64:67], a[120:123], a[64:67]\n"
      "buffer_load_dwordx4 v[56:59], v233, s[64:67], s80 offen offset:2560\n"
      "v_perm_b32 v145, v13, v9, s77\n"
      "v_cndmask_b32_dpp v68, v70, v144, vcc row_ror:8 row_mask:0xf bank_mask:0xf\n"
      "v_mfma_f32_16x16x32_bf16 a[80:83], v[64:67], a[124:127], a[80:83]\n"
      "ds_read_b128 a[168:171], v210 offset:0\n"
      "v_cndmask_b32_dpp v69, v71, v145, vcc row_ror:8 row_mask:0xf bank_mask:0xf\n"
      "v_mov_b32_dpp v70, v144 row_ror:8 row_mask:0xf bank_mask:0x3\n"
      "v_mov_b32_dpp v71, v145 row_ror:8 row_mask:0xf bank_mask:0x3\n"
      "v_mfma_f32_16x16x32_bf16 v[164:167], v[64:67], a[112:115], v[164:167]\n"
      "buffer_load_dwordx4 v[60:63], v233, s[64:67], s80 offen offset:2816\n"
      "v_perm_b32 v74, v6, v2, s78\n"
      "v_perm_b32 v75, v14, v10, s78\n"
      "v_perm_b32 v146, v6, v2, s77\n"
      "v_mfma_f32_16x16x32_bf16 a[52:55], v[68:71], a[116:119], a[52:55]\n"
      "ds_read_b128 a[172:175], v211 offset:0\n"
      "v_perm_b32 v147, v14, v10, s77\n"
      "v_cndmask_b32_dpp v72, v74, v146, vcc row_ror:8 row_mask:0xf bank_mask:0xf\n"
      "v_mfma_f32_16x16x32_bf16 a[68:71], v[68:71], a[120:123], a[68:71]\n"
      "buffer_load_dwordx4 v[128:131], v234, s[64:67], s83 offen offset:0\n"
      "v_cndmask_b32_dpp v73, v75, v147, vcc row_ror:8 row_mask:0xf bank_mask:0xf\n"
      "v_mov_b32_dpp v74, v146 row_ror:8 row_mask:0xf bank_mask:0x3\n"
      "v_mfma_f32_16x16x32_bf16 a[84:87], v[68:71], a[124:127], a[84:87]\n"
      "ds_read_b128 a[160:163], v208 offset:0\n"
      "v_mov_b32_dpp v75, v147 row_ror:8 row_mask:0xf bank_mask:0x3\n"
      "v_perm_b32 v78, v7, v3, s78\n"
      "v_perm_b32 v79, v15, v11, s78\n"
      "v_mfma_f32_16x16x32_bf16 v[168:171], v[68:71], a[112:115], v[168:171]\n"
      "buffer_load_dwordx4 v[132:135], v234, s[64:67], s83 offen offset:256\n"
      "v_perm_b32 v144, v7, v3, s77\n"
      "v_perm_b32 v145, v15, v11, s77\n"
      "v_mfma_f32_16x16x32_bf16 a[56:59], v[72:75], a[116:119], a[56:59]\n"
      "ds_read_b128 a[180:183], v201 offset:32768\n"
      "v_cndmask_b32_dpp v76, v78, v144, vcc row_ror:8 row_mask:0xf bank_mask:0xf\n"
      "v_cndmask_b32_dpp v77, v79, v145, vcc row_ror:8 row_mask:0xf bank_mask:0xf\n"
      "v_mfma_f32_16x16x32_bf16 a[72:75], v[72:75], a[120:123], a[72:75]\n"
      "buffer_load_dwordx4 v[136:139], v234, s[64:67], s83 offen offset:512\n"
      "v_mov_b32_dpp v78, v144 row_ror:8 row_mask:0xf bank_mask:0x3\n"
      "v_mov_b32_dpp v79, v145 row_ror:8 row_mask:0xf bank_mask:0x3\n"
      "v_mfma_f32_16x16x32_bf16 a[88:91], v[72:75], a[124:127], a[88:91]\n"
      "ds_read_b128 a[184:187], v202 offset:32768\n"
      "s_waitcnt vmcnt(11)\n"
      "v_perm_b32 v82, v20, v16, s78\n"
      "v_perm_b32 v83, v28, v24, s78\n"
      "v_perm_b32 v146, v20, v16, s77\n"
      "v_mfma_f32_16x16x32_bf16 v[172:175], v[72:75], a[112:115], v[172:175]\n"
      "buffer_load_dwordx4 v[140:143], v234, s[64:67], s83 offen offset:768\n"
      "v_perm_b32 v147, v28, v24, s77\n"
      "v_cndmask_b32_dpp v80, v82, v146, vcc row_ror:8 row_mask:0xf bank_mask:0xf\n"
      "v_mfma_f32_16x16x32_bf16 a[60:63], v[76:79], a[116:119], a[60:63]\n"
      "ds_read_b128 a[188:191], v203 offset:32768\n"
      "v_cndmask_b32_dpp v81, v83, v147, vcc row_ror:8 row_mask:0xf bank_mask:0xf\n"
      "v_mov_b32_dpp v82, v146 row_ror:8 row_mask:0xf bank_mask:0x3\n"
      "v_mfma_f32_16x16x32_bf16 a[76:79], v[76:79], a[120:123], a[76:79]\n"
      "v_mov_b32_dpp v83, v147 row_ror:8 row_mask:0xf bank_mask:0x3\n"
      "v_perm_b32 v86, v21, v17, s78\n"
      "v_mfma_f32_16x16x32_bf16 a[92:95], v[76:79], a[124:127], a[92:95]\n"
      "ds_read_b128 a[176:179], v200 offset:32768\n"
      "v_perm_b32 v87, v29, v25, s78\n"
      "v_perm_b32 v144, v21, v17, s77\n"
      "v_perm_b32 v145, v29, v25, s77\n"
      "v_mfma_f32_16x16x32_bf16 v[176:179], v[76:79], a[112:115], v[176:179]\n"
      "v_cndmask_b32_dpp v84, v86, v144, vcc row_ror:8 row_mask:0xf bank_mask:0xf\n"
      "v_cndmask_b32_dpp v85, v87, v145, vcc row_ror:8 row_mask:0xf bank_mask:0xf\n"
      "s_waitcnt lgkmcnt(4)\n"
      "v_mfma_f32_16x16x32_bf16 a[48:51], v[80:83], a[164:167], a[48:51]\n"
      "ds_read_b128 a[196:199], v209 offset:32768\n"
      "v_mov_b32_dpp v86, v144 row_ror:8 row_mask:0xf bank_mask:0x3\n"
      "v_mov_b32_dpp v87, v145 row_ror:8 row_mask:0xf bank_mask:0x3\n"
      "v_mfma_f32_16x16x32_bf16 a[64:67], v[80:83], a[168:171], a[64:67]\n"
      "v_perm_b32 v90, v22, v18, s78\n"
      "v_perm_b32 v91, v30, v26, s78\n"
      "v_mfma_f32_16x16x32_bf16 a[80:83], v[80:83], a[172:175], a[80:83]\n"
      "ds_read_b128 a[200:203], v210 offset:32768\n"
      "v_perm_b32 v146, v22, v18, s77\n"
      "v_perm_b32 v147, v30, v26, s77\n"
      "v_mfma_f32_16x16x32_bf16 v[164:167], v[80:83], a[160:163], v[164:167]\n"
      "v_cndmask_b32_dpp v88, v90, v146, vcc row_ror:8 row_mask:0xf bank_mask:0xf\n"
      "v_cndmask_b32_dpp v89, v91, v147, vcc row_ror:8 row_mask:0xf bank_mask:0xf\n"
      "v_mfma_f32_16x16x32_bf16 a[52:55], v[84:87], a[164:167], a[52:55]\n"
      "ds_read_b128 a[204:207], v211 offset:32768\n"
      "v_mov_b32_dpp v90, v146 row_ror:8 row_mask:0xf bank_mask:0x3\n"
      "v_mov_b32_dpp v91, v147 row_ror:8 row_mask:0xf bank_mask:0x3\n"
      "v_mfma_f32_16x16x32_bf16 a[68:71], v[84:87], a[168:171], a[68:71]\n"
      "v_perm_b32 v94, v23, v19, s78\n"
      "v_perm_b32 v95, v31, v27, s78\n"
      "v_mfma_f32_16x16x32_bf16 a[84:87], v[84:87], a[172:175], a[84:87]\n"
      "ds_read_b128 a[192:195], v208 offset:32768\n"
      "v_perm_b32 v144, v23, v19, s77\n"
      "v_perm_b32 v145, v31, v27, s77\n"
      "v_mfma_f32_16x16x32_bf16 v[168:171], v[84:87], a[160:163], v[168:171]\n"
      "v_cndmask_b32_dpp v92, v94, v144, vcc row_ror:8 row_mask:0xf bank_mask:0xf\n"
      "v_cndmask_b32_dpp v93, v95, v145, vcc row_ror:8 row_mask:0xf bank_mask:0xf\n"
      "v_mfma_f32_16x16x32_bf16 a[56:59], v[88:91], a[164:167], a[56:59]\n"
      "ds_read_b128 a[212:215], v217 offset:0\n"
      "v_mov_b32_dpp v94, v144 row_ror:8 row_mask:0xf bank_mask:0x3\n"
      "v_mov_b32_dpp v95, v145 row_ror:8 row_mask:0xf bank_mask:0x3\n"
      "v_mfma_f32_16x16x32_bf16 a[72:75], v[88:91], a[168:171], a[72:75]\n"
      "s_waitcnt vmcnt(8)\n"
      "v_perm_b32 v98, v36, v32, s78\n"
      "v_perm_b32 v99, v44, v40, s78\n"
      "v_mfma_f32_16x16x32_bf16 a[88:91], v[88:91], a[172:175], a[88:91]\n"
      "ds_read_b128 a[216:219], v218 offset:0\n"
      "v_perm_b32 v146, v36, v32, s77\n"
      "v_perm_b32 v147, v44, v40, s77\n"
      "v_mfma_f32_16x16x32_bf16 v[172:175], v[88:91], a[160:163], v[172:175]\n"
      "v_cndmask_b32_dpp v96, v98, v146, vcc row_ror:8 row_mask:0xf bank_mask:0xf\n"
      "v_cndmask_b32_dpp v97, v99, v147, vcc row_ror:8 row_mask:0xf bank_mask:0xf\n"
      "v_mfma_f32_16x16x32_bf16 a[60:63], v[92:95], a[164:167], a[60:63]\n"
      "ds_read_b128 a[220:223], v219 offset:0\n"
      "v_mov_b32_dpp v98, v146 row_ror:8 row_mask:0xf bank_mask:0x3\n"
      "v_mov_b32_dpp v99, v147 row_ror:8 row_mask:0xf bank_mask:0x3\n"
      "v_mfma_f32_16x16x32_bf16 a[76:79], v[92:95], a[168:171], a[76:79]\n"
      "s_waitcnt vmcnt(4)\n"
      "v_perm_b32 v114, v52, v48, s78\n"
      "v_perm_b32 v115, v60, v56, s78\n"
      "v_mfma_f32_16x16x32_bf16 a[92:95], v[92:95], a[172:175], a[92:95]\n"
      "ds_read_b128 a[208:211], v216 offset:0\n"
      "v_perm_b32 v146, v52, v48, s77\n"
      "v_perm_b32 v147, v60, v56, s77\n"
      "v_mfma_f32_16x16x32_bf16 v[176:179], v[92:95], a[160:163], v[176:179]\n"
      "v_cndmask_b32_dpp v112, v114, v146, vcc row_ror:8 row_mask:0xf bank_mask:0xf\n"
      "v_cndmask_b32_dpp v113, v115, v147, vcc row_ror:8 row_mask:0xf bank_mask:0xf\n"
      "s_waitcnt lgkmcnt(8)\n"
      "v_mfma_f32_16x16x32_bf16 a[48:51], v[96:99], a[180:183], a[48:51]\n"
      "ds_read_b128 a[224:227], v205 offset:0\n"
      "v_mov_b32_dpp v114, v146 row_ror:8 row_mask:0xf bank_mask:0x3\n"
      "v_mov_b32_dpp v115, v147 row_ror:8 row_mask:0xf bank_mask:0x3\n"
      "v_mfma_f32_16x16x32_bf16 a[64:67], v[96:99], a[184:187], a[64:67]\n"
      "s_waitcnt vmcnt(0)\n"
      "v_perm_b32 v2, v132, v128, s78\n"
      "v_perm_b32 v3, v140, v136, s78\n"
      "v_mfma_f32_16x16x32_bf16 a[80:83], v[96:99], a[188:191], a[80:83]\n"
      "ds_read_b128 a[228:231], v197 offset:32768\n"
      "v_perm_b32 v144, v132, v128, s77\n"
      "v_perm_b32 v145, v140, v136, s77\n"
      "v_mfma_f32_16x16x32_bf16 v[164:167], v[96:99], a[176:179], v[164:167]\n"
      "v_cndmask_b32_dpp v0, v2, v144, vcc row_ror:8 row_mask:0xf bank_mask:0xf\n"
      "v_cndmask_b32_dpp v1, v3, v145, vcc row_ror:8 row_mask:0xf bank_mask:0xf\n"
      "s_waitcnt lgkmcnt(6)\n"
      "v_mfma_f32_16x16x32_bf16 a[48:51], v[112:115], a[196:199], a[48:51]\n"
      "ds_read_b128 a[232:235], v205 offset:32768\n"
      "v_mov_b32_dpp v2, v144 row_ror:8 row_mask:0xf bank_mask:0x3\n"
      "v_mov_b32_dpp v3, v145 row_ror:8 row_mask:0xf bank_mask:0x3\n"
      "v_mfma_f32_16x16x32_bf16 a[64:67], v[112:115], a[200:203], a[64:67]\n"
      "v_perm_b32 v102, v37, v33, s78\n"
      "v_perm_b32 v103, v45, v41, s78\n"
      "v_mfma_f32_16x16x32_bf16 a[80:83], v[112:115], a[204:207], a[80:83]\n"
      "ds_read_b128 a[236:239], v213 offset:0\n"
      "v_perm_b32 v146, v37, v33, s77\n"
      "v_perm_b32 v147, v45, v41, s77\n"
      "v_mfma_f32_16x16x32_bf16 v[164:167], v[112:115], a[192:195], v[164:167]\n"
      "v_cndmask_b32_dpp v100, v102, v146, vcc row_ror:8 row_mask:0xf bank_mask:0xf\n"
      "v_cndmask_b32_dpp v101, v103, v147, vcc row_ror:8 row_mask:0xf bank_mask:0xf\n"
      "s_waitcnt lgkmcnt(4)\n"
      "v_mfma_f32_16x16x32_bf16 a[48:51], v[0:3], a[212:215], a[48:51]\n"
      "ds_read_b128 a[240:243], v206 offset:0\n"
      "v_mov_b32_dpp v102, v146 row_ror:8 row_mask:0xf bank_mask:0x3\n"
      "v_mov_b32_dpp v103, v147 row_ror:8 row_mask:0xf bank_mask:0x3\n"
      "v_mfma_f32_16x16x32_bf16 a[64:67], v[0:3], a[216:219], a[64:67]\n"
      "s_mov_b64 exec, 1\n"
      "global_load_dwordx2 v[246:247], v243, s[38:39] offset:256 sc1\n"
      "s_mov_b64 exec, -1\n"
      "v_mfma_f32_16x16x32_bf16 a[80:83], v[0:3], a[220:223], a[80:83]\n"
      "ds_read_b128 a[244:247], v198 offset:32768\n"
      "v_perm_b32 v118, v53, v49, s78\n"
      "v_perm_b32 v119, v61, v57, s78\n"
      "v_perm_b32 v144, v53, v49, s77\n"
      "v_mfma_f32_16x16x32_bf16 v[164:167], v[0:3], a[208:211], v[164:167]\n"
      "v_perm_b32 v145, v61, v57, s77\n"
      "v_cndmask_b32_dpp v116, v118, v144, vcc row_ror:8 row_mask:0xf bank_mask:0xf\n"
      "v_mfma_f32_16x16x32_bf16 a[52:55], v[100:103], a[180:183], a[52:55]\n"
      "ds_write_b128 v236, a[48:51] offset:0\n"
      "v_mfma_f32_16x16x32_bf16 a[68:71], v[100:103], a[184:187], a[68:71]\n"
      "v_cndmask_b32_dpp v117, v119, v145, vcc row_ror:8 row_mask:0xf bank_mask:0xf\n"
      "v_mov_b32_dpp v118, v144 row_ror:8 row_mask:0xf bank_mask:0x3\n"
      "v_mfma_f32_16x16x32_bf16 a[84:87], v[100:103], a[188:191], a[84:87]\n"
      "ds_read_b128 a[248:251], v206 offset:32768\n"
      "v_mov_b32_dpp v119, v145 row_ror:8 row_mask:0xf bank_mask:0x3\n"
      "v_perm_b32 v6, v133, v129, s78\n"
      "v_perm_b32 v7, v141, v137, s78\n"
      "v_mfma_f32_16x16x32_bf16 v[168:171], v[100:103], a[176:179], v[168:171]\n"
      "v_perm_b32 v146, v133, v129, s77\n"
      "v_perm_b32 v147, v141, v137, s77\n"
      "v_mfma_f32_16x16x32_bf16 a[52:55], v[116:119], a[196:199], a[52:55]\n"
      "ds_write_b128 v237, a[64:67] offset:0\n"
      "v_mfma_f32_16x16x32_bf16 a[68:71], v[116:119], a[200:203], a[68:71]\n"
      "v_cndmask_b32_dpp v4, v6, v146, vcc row_ror:8 row_mask:0xf bank_mask:0xf\n"
      "v_cndmask_b32_dpp v5, v7, v147, vcc row_ror:8 row_mask:0xf bank_mask:0xf\n"
      "v_mfma_f32_16x16x32_bf16 a[84:87], v[116:119], a[204:207], a[84:87]\n"
      "ds_read_b128 a[252:255], v214 offset:0\n"
      "v_mov_b32_dpp v6, v146 row_ror:8 row_mask:0xf bank_mask:0x3\n"
      "v_mov_b32_dpp v7, v147 row_ror:8 row_mask:0xf bank_mask:0x3\n"
      "v_perm_b32 v106, v38, v34, s78\n"
      "v_mfma_f32_16x16x32_bf16 v[168:171], v[116:119], a[192:195], v[168:171]\n"
      "v_perm_b32 v107, v46, v42, s78\n"
      "v_perm_b32 v144, v38, v34, s77\n"
      "v_perm_b32 v145, v46, v42, s77\n"
      "v_mfma_f32_16x16x32_bf16 a[52:55], v[4:7], a[212:215], a[52:55]\n"
      "ds_write_b128 v238, a[80:83] offset:0\n"
      "v_mfma_f32_16x16x32_bf16 a[68:71], v[4:7], a[216:219], a[68:71]\n"
      "v_cndmask_b32_dpp v104, v106, v144, vcc row_ror:8 row_mask:0xf bank_mask:0xf\n"
      "v_cndmask_b32_dpp v105, v107, v145, vcc row_ror:8 row_mask:0xf bank_mask:0xf\n"
      "v_mfma_f32_16x16x32_bf16 a[84:87], v[4:7], a[220:223], a[84:87]\n"
      "ds_read_b128 a[128:131], v207 offset:0\n"
      "v_mov_b32_dpp v106, v144 row_ror:8 row_mask:0xf bank_mask:0x3\n"
      "v_mov_b32_dpp v107, v145 row_ror:8 row_mask:0xf bank_mask:0x3\n"
      "v_perm_b32 v122, v54, v50, s78\n"
      "v_mfma_f32_16x16x32_bf16 v[168:171], v[4:7], a[208:211], v[168:171]\n"
      "v_perm_b32 v123, v62, v58, s78\n"
      "v_perm_b32 v146, v54, v50, s77\n"
      "v_perm_b32 v147, v62, v58, s77\n"
      "v_mfma_f32_16x16x32_bf16 a[56:59], v[104:107], a[180:183], a[56:59]\n"
      "ds_write_b128 v236, a[52:55] offset:1024\n"
      "v_mfma_f32_16x16x32_bf16 a[72:75], v[104:107], a[184:187], a[72:75]\n"
      "v_cndmask_b32_dpp v120, v122, v146, vcc row_ror:8 row_mask:0xf bank_mask:0xf\n"
      "v_cndmask_b32_dpp v121, v123, v147, vcc row_ror:8 row_mask:0xf bank_mask:0xf\n"
      "v_mfma_f32_16x16x32_bf16 a[88:91], v[104:107], a[188:191], a[88:91]\n"
      "ds_read_b128 a[132:135], v199 offset:32768\n"
      "v_mov_b32_dpp v122, v146 row_ror:8 row_mask:0xf bank_mask:0x3\n"
      "v_mov_b32_dpp v123, v147 row_ror:8 row_mask:0xf bank_mask:0x3\n"
      "v_perm_b32 v10, v134, v130, s78\n"
      "v_mfma_f32_16x16x32_bf16 v[172:175], v[104:107], a[176:179], v[172:175]\n"
      "v_perm_b32 v11, v142, v138, s78\n"
      "v_perm_b32 v144, v134, v130, s77\n"
      "v_perm_b32 v145, v142, v138, s77\n"
      "v_mfma_f32_16x16x32_bf16 a[56:59], v[120:123], a[196:199], a[56:59]\n"
      "ds_write_b128 v237, a[68:71] offset:1024\n"
      "v_mfma_f32_16x16x32_bf16 a[72:75], v[120:123], a[200:203], a[72:75]\n"
      "v_cndmask_b32_dpp v8, v10, v144, vcc row_ror:8 row_mask:0xf bank_mask:0xf\n"
      "v_cndmask_b32_dpp v9, v11, v145, vcc row_ror:8 row_mask:0xf bank_mask:0xf\n"
      "v_mfma_f32_16x16x32_bf16 a[88:91], v[120:123], a[204:207], a[88:91]\n"
      "ds_read_b128 a[136:139], v207 offset:32768\n"
      "v_mov_b32_dpp v10, v144 row_ror:8 row_mask:0xf bank_mask:0x3\n"
      "v_mov_b32_dpp v11, v145 row_ror:8 row_mask:0xf bank_mask:0x3\n"
      "v_perm_b32 v110, v39, v35, s78\n"
      "v_mfma_f32_16x16x32_bf16 v[172:175], v[120:123], a[192:195], v[172:175]\n"
      "v_perm_b32 v111, v47, v43, s78\n"
      "v_perm_b32 v144, v39, v35, s77\n"
      "v_perm_b32 v145, v47, v43, s77\n"
      "v_mfma_f32_16x16x32_bf16 a[56:59], v[8:11], a[212:215], a[56:59]\n"
      "ds_write_b128 v238, a[84:87] offset:1024\n"
      "v_mfma_f32_16x16x32_bf16 a[72:75], v[8:11], a[216:219], a[72:75]\n"
      "v_cndmask_b32_dpp v108, v110, v144, vcc row_ror:8 row_mask:0xf bank_mask:0xf\n"
      "v_cndmask_b32_dpp v109, v111, v145, vcc row_ror:8 row_mask:0xf bank_mask:0xf\n"
      "v_mfma_f32_16x16x32_bf16 a[88:91], v[8:11], a[220:223], a[88:91]\n"
      "ds_read_b128 a[140:143], v215 offset:0\n"
      "v_mov_b32_dpp v110, v144 row_ror:8 row_mask:0xf bank_mask:0x3\n"
      "v_mov_b32_dpp v111, v145 row_ror:8 row_mask:0xf bank_mask:0x3\n"
      "v_perm_b32 v126, v55, v51, s78\n"
      "v_mfma_f32_16x16x32_bf16 v[172:175], v[8:11], a[208:211], v[172:175]\n"
      "v_perm_b32 v127, v63, v59, s78\n"
      "v_perm_b32 v146, v55, v51, s77\n"
      "v_perm_b32 v147, v63, v59, s77\n"
      "v_mfma_f32_16x16x32_bf16 a[60:63], v[108:111], a[180:183], a[60:63]\n"
      "ds_write_b128 v236, a[56:59] offset:2048\n"
      "v_mfma_f32_16x16x32_bf16 a[76:79], v[108:111], a[184:187], a[76:79]\n"
      "v_cndmask_b32_dpp v124, v126, v146, vcc row_ror:8 row_mask:0xf bank_mask:0xf\n"
      "v_cndmask_b32_dpp v125, v127, v147, vcc row_ror:8 row_mask:0xf bank_mask:0xf\n"
      "v_mfma_f32_16x16x32_bf16 a[92:95], v[108:111], a[188:191], a[92:95]\n"
      "v_mov_b32_dpp v126, v146 row_ror:8 row_mask:0xf bank_mask:0x3\n"
      "v_mov_b32_dpp v127, v147 row_ror:8 row_mask:0xf bank_mask:0x3\n"
      "v_perm_b32 v14, v135, v131, s78\n"
      "v_mfma_f32_16x16x32_bf16 v[176:179], v[108:111], a[176:179], v[176:179]\n"
      "v_perm_b32 v15, v143, v139, s78\n"
      "v_perm_b32 v144, v135, v131, s77\n"
      "v_perm_b32 v145, v143, v139, s77\n"
      "v_mfma_f32_16x16x32_bf16 a[60:63], v[124:127], a[196:199], a[60:63]\n"
      "ds_write_b128 v237, a[72:75] offset:2048\n"
      "v_mfma_f32_16x16x32_bf16 a[76:79], v[124:127], a[200:203], a[76:79]\n"
      "v_cndmask_b32_dpp v12, v14, v144, vcc row_ror:8 row_mask:0xf bank_mask:0xf\n"
      "v_cndmask_b32_dpp v13, v15, v145, vcc row_ror:8 row_mask:0xf bank_mask:0xf\n"
      "v_mfma_f32_16x16x32_bf16 a[92:95], v[124:127], a[204:207], a[92:95]\n"
      "v_mov_b32_dpp v14, v144 row_ror:8 row_mask:0xf bank_mask:0x3\n"
      "v_mov_b32_dpp v15, v145 row_ror:8 row_mask:0xf bank_mask:0x3\n"
      "v_mfma_f32_16x16x32_bf16 v[176:179], v[124:127], a[192:195], v[176:179]\n"
      "s_nop 0\n"
      "v_mfma_f32_16x16x32_bf16 a[60:63], v[12:15], a[212:215], a[60:63]\n"
      "ds_write_b128 v238, a[88:91] offset:2048\n"
      "v_mfma_f32_16x16x32_bf16 a[76:79], v[12:15], a[216:219], a[76:79]\n"
      "s_waitcnt lgkmcnt(4)\n"
      "s_barrier\n"
      "v_mfma_f32_16x16x32_bf16 a[92:95], v[12:15], a[220:223], a[92:95]\n"
      "ds_read_b128 v[16:19], v239 offset:0\n"
      "ds_read_b128 v[20:23], v239 offset:4096\n"
      "v_mfma_f32_16x16x32_bf16 v[176:179], v[12:15], a[208:211], v[176:179]\n"
      "ds_read_b128 v[24:27], v239 offset:8192\n"
      "ds_read_b128 v[28:31], v239 offset:1024\n"
      "v_mfma_f32_16x16x32_bf16 a[0:3], v[64:67], a[100:103], a[0:3]\n"
      "ds_write_b128 v236, a[60:63] offset:3072\n"
      "v_mfma_f32_16x16x32_bf16 a[4:7], v[68:71], a[100:103], a[4:7]\n"
      "v_mfma_f32_16x16x32_bf16 a[0:3], v[80:83], a[224:227], a[0:3]\n"
      "ds_read_b128 v[32:35], v239 offset:5120\n"
      "ds_read_b128 v[36:39], v239 offset:9216\n"
      "v_mfma_f32_16x16x32_bf16 a[4:7], v[84:87], a[224:227], a[4:7]\n"
      "ds_write_b128 v237, a[76:79] offset:3072\n"
      "v_mfma_f32_16x16x32_bf16 a[0:3], v[96:99], a[228:231], a[0:3]\n"
      "v_mfma_f32_16x16x32_bf16 a[4:7], v[100:103], a[228:231], a[4:7]\n"
      "v_mfma_f32_16x16x32_bf16 a[0:3], v[112:115], a[232:235], a[0:3]\n"
      "ds_write_b128 v238, a[92:95] offset:3072\n"
      "v_mfma_f32_16x16x32_bf16 a[4:7], v[116:119], a[232:235], a[4:7]\n"
      "s_waitcnt lgkmcnt(6)\n"
      "v_add_f32 v16, v16, v20\n"
      "v_add_f32 v17, v17, v21\n"
      "v_mfma_f32_16x16x32_bf16 a[0:3], v[0:3], a[236:239], a[0:3]\n"
      "v_add_f32 v18, v18, v22\n"
      "v_add_f32 v19, v19, v23\n"
      "v_add_f32 v24, v24, v164\n"
      "v_mfma_f32_16x16x32_bf16 a[4:7], v[4:7], a[236:239], a[4:7]\n"
      "v_add_f32 v25, v25, v165\n"
      "v_add_f32 v26, v26, v166\n"
      "v_add_f32 v27, v27, v167\n"
      "v_mfma_f32_16x16x32_bf16 a[8:11], v[72:75], a[100:103], a[8:11]\n"
      "v_add_f32 v16, v16, v24\n"
      "v_add_f32 v17, v17, v25\n"
      "v_add_f32 v18, v18, v26\n"
      "v_mfma_f32_16x16x32_bf16 a[12:15], v[76:79], a[100:103], a[12:15]\n"
      "v_add_f32 v19, v19, v27\n"
      "s_waitcnt lgkmcnt(2)\n"
      "v_add_f32 v28, v28, v32\n"
      "v_mfma_f32_16x16x32_bf16 a[8:11], v[88:91], a[224:227], a[8:11]\n"
      "ds_write_b128 v236, a[0:3] offset:49152\n"
      "v_mfma_f32_16x16x32_bf16 a[12:15], v[92:95], a[224:227], a[12:15]\n"
      "v_add_f32 v29, v29, v33\n"
      "v_add_f32 v30, v30, v34\n"
      "v_add_f32 v31, v31, v35\n"
      "v_mfma_f32_16x16x32_bf16 a[8:11], v[104:107], a[228:231], a[8:11]\n"
      "v_add_f32 v36, v36, v168\n"
      "v_add_f32 v37, v37, v169\n"
      "v_add_f32 v38, v38, v170\n"
      "v_mfma_f32_16x16x32_bf16 a[12:15], v[108:111], a[228:231], a[12:15]\n"
      "ds_write_b128 v236, a[4:7] offset:50176\n"
      "v_mfma_f32_16x16x32_bf16 a[8:11], v[120:123], a[232:235], a[8:11]\n"
      "s_waitcnt lgkmcnt(2)\n"
      "s_barrier\n"
      "v_add_f32 v39, v39, v171\n"
      "v_mfma_f32_16x16x32_bf16 a[12:15], v[124:127], a[232:235], a[12:15]\n"
      "ds_read_b128 v[40:43], v239 offset:2048\n"
      "ds_read_b128 v[44:47], v239 offset:6144\n"
      "v_add_f32 v28, v28, v36\n"
      "v_mfma_f32_16x16x32_bf16 a[8:11], v[8:11], a[236:239], a[8:11]\n"
      "ds_read_b128 v[48:51], v239 offset:10240\n"
      "ds_read_b128 v[52:55], v239 offset:3072\n"
      "v_add_f32 v29, v29, v37\n"
      "v_mfma_f32_16x16x32_bf16 a[12:15], v[12:15], a[236:239], a[12:15]\n"
      "ds_read_b128 v[56:59], v239 offset:7168\n"
      "ds_read_b128 v[60:63], v239 offset:11264\n"
      "v_add_f32 v30, v30, v38\n"
      "v_mfma_f32_16x16x32_bf16 a[16:19], v[64:67], a[104:107], a[16:19]\n"
      "v_add_f32 v31, v31, v39\n"
      "v_cvt_pk_bf16_f32 v128, v16, v28\n"
      "v_cvt_pk_bf16_f32 v130, v17, v29\n"
      "v_mfma_f32_16x16x32_bf16 a[20:23], v[68:71], a[104:107], a[20:23]\n"
      "v_cvt_pk_bf16_f32 v132, v18, v30\n"
      "v_cvt_pk_bf16_f32 v134, v19, v31\n"
      "v_pk_max_i16 v128, v128, 0\n"
      "v_mfma_f32_16x16x32_bf16 a[16:19], v[80:83], a[240:243], a[16:19]\n"
      "ds_write_b128 v236, a[8:11] offset:51200\n"
      "v_mfma_f32_16x16x32_bf16 a[20:23], v[84:87], a[240:243], a[20:23]\n"
      "v_pk_max_i16 v130, v130, 0\n"
      "v_pk_max_i16 v132, v132, 0\n"
      "v_pk_max_i16 v134, v134, 0\n"
      "v_mfma_f32_16x16x32_bf16 a[16:19], v[96:99], a[244:247], a[16:19]\n"
      "v_mfma_f32_16x16x32_bf16 a[20:23], v[100:103], a[244:247], a[20:23]\n"
      "ds_write_b128 v236, a[12:15] offset:52224\n"
      "v_mfma_f32_16x16x32_bf16 a[16:19], v[112:115], a[248:251], a[16:19]\n"
      "s_waitcnt lgkmcnt(5)\n"
      "v_add_f32 v40, v40, v44\n"
      "v_add_f32 v41, v41, v45\n"
      "v_mfma_f32_16x16x32_bf16 a[20:23], v[116:119], a[248:251], a[20:23]\n"
      "v_add_f32 v42, v42, v46\n"
      "v_add_f32 v43, v43, v47\n"
      "v_add_f32 v48, v48, v172\n"
      "v_mfma_f32_16x16x32_bf16 a[16:19], v[0:3], a[252:255], a[16:19]\n"
      "ds_read_b128 a[144:147], v204 offset:0\n"
      "v_add_f32 v49, v49, v173\n"
      "v_add_f32 v50, v50, v174\n"
      "v_mfma_f32_16x16x32_bf16 a[20:23], v[4:7], a[252:255], a[20:23]\n"
      "v_add_f32 v51, v51, v175\n"
      "v_add_f32 v40, v40, v48\n"
      "v_add_f32 v41, v41, v49\n"
      "v_mfma_f32_16x16x32_bf16 a[24:27], v[72:75], a[104:107], a[24:27]\n"
      "ds_read_b128 a[148:151], v196 offset:32768\n"
      "v_add_f32 v42, v42, v50\n"
      "v_add_f32 v43, v43, v51\n"
      "v_mfma_f32_16x16x32_bf16 a[28:31], v[76:79], a[104:107], a[28:31]\n"
      "s_waitcnt lgkmcnt(4)\n"
      "v_add_f32 v52, v52, v56\n"
      "v_add_f32 v53, v53, v57\n"
      "v_mfma_f32_16x16x32_bf16 a[24:27], v[88:91], a[240:243], a[24:27]\n"
      "ds_write_b128 v237, a[16:19] offset:49152\n"
      "v_mfma_f32_16x16x32_bf16 a[28:31], v[92:95], a[240:243], a[28:31]\n"
      "v_add_f32 v54, v54, v58\n"
      "v_add_f32 v55, v55, v59\n"
      "v_add_f32 v60, v60, v176\n"
      "v_mfma_f32_16x16x32_bf16 a[24:27], v[104:107], a[244:247], a[24:27]\n"
      "ds_read_b128 a[152:155], v204 offset:32768\n"
      "v_add_f32 v61, v61, v177\n"
      "v_add_f32 v62, v62, v178\n"
      "v_mfma_f32_16x16x32_bf16 a[28:31], v[108:111], a[244:247], a[28:31]\n"
      "ds_write_b128 v237, a[20:23] offset:50176\n"
      "v_mfma_f32_16x16x32_bf16 a[24:27], v[120:123], a[248:251], a[24:27]\n"
      "v_add_f32 v63, v63, v179\n"
      "v_add_f32 v52, v52, v60\n"
      "v_add_f32 v53, v53, v61\n"
      "v_mfma_f32_16x16x32_bf16 a[28:31], v[124:127], a[248:251], a[28:31]\n"
      "ds_read_b128 a[156:159], v212 offset:0\n"
      "v_add_f32 v54, v54, v62\n"
      "v_add_f32 v55, v55, v63\n"
      "v_mfma_f32_16x16x32_bf16 a[24:27], v[8:11], a[252:255], a[24:27]\n"
      "v_cvt_pk_bf16_f32 v129, v40, v52\n"
      "v_cvt_pk_bf16_f32 v131, v41, v53\n"
      "v_cvt_pk_bf16_f32 v133, v42, v54\n"
      "v_mfma_f32_16x16x32_bf16 a[28:31], v[12:15], a[252:255], a[28:31]\n"
      "v_cvt_pk_bf16_f32 v135, v43, v55\n"
      "v_pk_max_i16 v129, v129, 0\n"
      "v_pk_max_i16 v131, v131, 0\n"
      "v_mfma_f32_16x16x32_bf16 a[32:35], v[64:67], a[108:111], a[32:35]\n"
      "v_pk_max_i16 v133, v133, 0\n"
      "v_pk_max_i16 v135, v135, 0\n"
      "v_mfma_f32_16x16x32_bf16 a[36:39], v[68:71], a[108:111], a[36:39]\n"
      "v_mfma_f32_16x16x32_bf16 v[192:195], v[180:183], v[128:131], 0\n"
      "v_mfma_f32_16x16x32_bf16 v[192:195], v[184:187], v[132:135], v[192:195]\n"
      "s_waitcnt vmcnt(0)\n"
      "v_mfma_f32_16x16x32_bf16 a[32:35], v[80:83], a[128:131], a[32:35]\n"
      "ds_write_b128 v237, a[24:27] offset:51200\n"
      "v_mfma_f32_16x16x32_bf16 a[36:39], v[84:87], a[128:131], a[36:39]\n"
      "v_readfirstlane_b32 s90, v247\n"
      "v_mfma_f32_16x16x32_bf16 a[32:35], v[96:99], a[132:135], a[32:35]\n"
      "v_mfma_f32_16x16x32_bf16 a[36:39], v[100:103], a[132:135], a[36:39]\n"
      "ds_write_b128 v237, a[28:31] offset:52224\n"
      "v_mfma_f32_16x16x32_bf16 a[32:35], v[112:115], a[136:139], a[32:35]\n"
      "v_mfma_f32_16x16x32_bf16 a[36:39], v[116:119], a[136:139], a[36:39]\n"
      "v_mov_b32 v193, s90\n"
      "v_writelane_b32 v192, s88, 16\n"
      "v_mfma_f32_16x16x32_bf16 a[32:35], v[0:3], a[140:143], a[32:35]\n"
      "s_mov_b64 exec, 0x1ffff\n"
      "global_store_dwordx2 v243, v[192:193], s[38:39] sc1\n"
      "s_mov_b64 exec, -1\n"
      "v_mfma_f32_16x16x32_bf16 a[36:39], v[4:7], a[140:143], a[36:39]\n"
      "v_mfma_f32_16x16x32_bf16 a[40:43], v[72:75], a[108:111], a[40:43]\n"
      "v_mfma_f32_16x16x32_bf16 a[44:47], v[76:79], a[108:111], a[44:47]\n"
      "v_mfma_f32_16x16x32_bf16 a[40:43], v[88:91], a[128:131], a[40:43]\n"
      "s_waitcnt lgkmcnt(0)\n"
      "s_barrier\n"
      "ds_write_b128 v238, a[32:35] offset:49152\n"
      "v_mfma_f32_16x16x32_bf16 a[44:47], v[92:95], a[128:131], a[44:47]\n"
      "v_mfma_f32_16x16x32_bf16 a[40:43], v[104:107], a[132:135], a[40:43]\n"
      "ds_read_b128 v[16:19], v239 offset:49152\n"
      "ds_read_b128 v[20:23], v239 offset:53248\n"
      "v_mfma_f32_16x16x32_bf16 a[44:47], v[108:111], a[132:135], a[44:47]\n"
      "ds_write_b128 v238, a[36:39] offset:50176\n"
      "v_mfma_f32_16x16x32_bf16 a[40:43], v[120:123], a[136:139], a[40:43]\n"
      "v_mfma_f32_16x16x32_bf16 a[44:47], v[124:127], a[136:139], a[44:47]\n"
      "ds_read_b128 v[28:31], v239 offset:50176\n"
      "ds_read_b128 v[32:35], v239 offset:54272\n"
      "v_mfma_f32_16x16x32_bf16 a[40:43], v[8:11], a[140:143], a[40:43]\n"
      "ds_read_b128 v[40:43], v239 offset:51200\n"
      "ds_read_b128 v[44:47], v239 offset:55296\n"
      "v_mfma_f32_16x16x32_bf16 a[44:47], v[12:15], a[140:143], a[44:47]\n"
      "ds_read_b128 v[52:55], v239 offset:52224\n"
      "ds_read_b128 v[56:59], v239 offset:56320\n"
      "v_mfma_f32_16x16x32_bf16 v[148:151], v[64:67], a[96:99], v[148:151]\n"
      "v_mfma_f32_16x16x32_bf16 v[152:155], v[68:71], a[96:99], v[152:155]\n"
      "v_mfma_f32_16x16x32_bf16 v[148:151], v[80:83], a[144:147], v[148:151]\n"
      "ds_write_b128 v238, a[40:43] offset:51200\n"
      "v_mfma_f32_16x16x32_bf16 v[152:155], v[84:87], a[144:147], v[152:155]\n"
      "s_waitcnt lgkmcnt(8)\n"
      "v_add_f32 v16, v16, v20\n"
      "v_mfma_f32_16x16x32_bf16 v[148:151], v[96:99], a[148:151], v[148:151]\n"
      "v_add_f32 v17, v17, v21\n"
      "v_add_f32 v18, v18, v22\n"
      "v_mfma_f32_16x16x32_bf16 v[152:155], v[100:103], a[148:151], v[152:155]\n"
      "ds_write_b128 v238, a[44:47] offset:52224\n"
      "v_mfma_f32_16x16x32_bf16 v[148:151], v[112:115], a[152:155], v[148:151]\n"
      "v_add_f32 v19, v19, v23\n"
      "s_waitcnt lgkmcnt(6)\n"
      "v_mfma_f32_16x16x32_bf16 v[152:155], v[116:119], a[152:155], v[152:155]\n"
      "v_add_f32 v28, v28, v32\n"
      "v_add_f32 v29, v29, v33\n"
      "v_mfma_f32_16x16x32_bf16 v[148:151], v[0:3], a[156:159], v[148:151]\n"
      "v_add_f32 v30, v30, v34\n"
      "v_add_f32 v31, v31, v35\n"
      "v_mfma_f32_16x16x32_bf16 v[152:155], v[4:7], a[156:159], v[152:155]\n"
      "s_waitcnt lgkmcnt(4)\n"
      "v_add_f32 v40, v40, v44\n"
      "v_mfma_f32_16x16x32_bf16 v[156:159], v[72:75], a[96:99], v[156:159]\n"
      "v_add_f32 v41, v41, v45\n"
      "v_add_f32 v42, v42, v46\n"
      "v_mfma_f32_16x16x32_bf16 v[160:163], v[76:79], a[96:99], v[160:163]\n"
      "s_waitcnt lgkmcnt(0)\n"
      "s_barrier\n"
      "ds_read_b128 v[24:27], v239 offset:57344\n"
      "v_mfma_f32_16x16x32_bf16 v[156:159], v[88:91], a[144:147], v[156:159]\n"
      "ds_read_b128 v[36:39], v239 offset:58368\n"
      "ds_read_b128 v[48:51], v239 offset:59392\n"
      "v_mfma_f32_16x16x32_bf16 v[160:163], v[92:95], a[144:147], v[160:163]\n"
      "ds_read_b128 v[60:63], v239 offset:60416\n"
      "v_add_f32 v43, v43, v47\n"
      "v_mfma_f32_16x16x32_bf16 v[156:159], v[104:107], a[148:151], v[156:159]\n"
      "s_mov_b64 exec, 0x1ffff\n"
      "global_load_dwordx2 v[244:245], v243, s[36:37] sc1\n"
      "s_mov_b64 exec, -1\n"
      "v_mfma_f32_16x16x32_bf16 v[160:163], v[108:111], a[148:151], v[160:163]\n"
      "v_add_f32 v52, v52, v56\n"
      "v_add_f32 v53, v53, v57\n"
      "v_mfma_f32_16x16x32_bf16 v[156:159], v[120:123], a[152:155], v[156:159]\n"
      "v_add_f32 v54, v54, v58\n"
      "v_add_f32 v55, v55, v59\n"
      "v_mfma_f32_16x16x32_bf16 v[160:163], v[124:127], a[152:155], v[160:163]\n"
      "v_add_f32 v16, v16, v148\n"
      "v_add_f32 v17, v17, v149\n"
      "v_mfma_f32_16x16x32_bf16 v[156:159], v[8:11], a[156:159], v[156:159]\n"
      "v_add_f32 v18, v18, v150\n"
      "v_add_f32 v19, v19, v151\n"
      "v_mfma_f32_16x16x32_bf16 v[160:163], v[12:15], a[156:159], v[160:163]\n"
      "v_add_f32 v28, v28, v152\n"
      "v_add_f32 v29, v29, v153\n"
      "v_add_f32 v30, v30, v154\n"
      "v_add_f32 v31, v31, v155\n"
      "s_lshl_b32 s91, s87, 5\n"
      "v_lshlrev_b32 v248, 1, v241\n"
      "v_add_u32 v248, s91, v248\n"
      "v_pk_add_f32 v[40:41], v[40:41], v[156:157]\n"
      "v_pk_add_f32 v[42:43], v[42:43], v[158:159]\n"
      "s_waitcnt lgkmcnt(3)\n"
      "v_pk_add_f32 v[16:17], v[16:17], v[24:25]\n"
      "v_pk_add_f32 v[18:19], v[18:19], v[26:27]\n"
      "s_waitcnt lgkmcnt(2)\n"
      "v_pk_add_f32 v[28:29], v[28:29], v[36:37]\n"
      "v_pk_add_f32 v[30:31], v[30:31], v[38:39]\n"
      "v_cvt_pk_bf16_f32 v128, v16, v28\n"
      "v_cvt_pk_bf16_f32 v130, v17, v29\n"
      "v_cvt_pk_bf16_f32 v132, v18, v30\n"
      "v_cvt_pk_bf16_f32 v134, v19, v31\n"
      "v_pk_max_i16 v128, v128, 0\n"
      "v_pk_max_i16 v130, v130, 0\n"
      "v_pk_max_i16 v132, v132, 0\n"
      "v_pk_max_i16 v134, v134, 0\n"
      "s_waitcnt lgkmcnt(1)\n"
      "v_pk_add_f32 v[40:41], v[40:41], v[48:49]\n"
      "v_pk_add_f32 v[42:43], v[42:43], v[50:51]\n"
      "s_waitcnt lgkmcnt(0)\n"
      "v_pk_add_f32 v[52:53], v[52:53], v[60:61]\n"
      "v_pk_add_f32 v[54:55], v[54:55], v[62:63]\n"
      "v_pk_add_f32 v[52:53], v[52:53], v[160:161]\n"
      "v_pk_add_f32 v[54:55], v[54:55], v[162:163]\n"
      "v_cvt_pk_bf16_f32 v129, v40, v52\n"
      "v_cvt_pk_bf16_f32 v131, v41, v53\n"
      "v_cvt_pk_bf16_f32 v133, v42, v54\n"
      "v_cvt_pk_bf16_f32 v135, v43, v55\n"
      "v_pk_max_i16 v129, v129, 0\n"
      "v_pk_max_i16 v131, v131, 0\n"
      "v_pk_max_i16 v133, v133, 0\n"
      "v_pk_max_i16 v135, v135, 0\n"
      "s_nop 0\n"
      "v_mfma_f32_16x16x32_bf16 v[188:191], v[180:183], v[128:131], v[188:191]\n"
      "v_mfma_f32_16x16x32_bf16 v[188:191], v[184:187], v[132:135], v[188:191]\n"
      "s_mov_b64 exec, 0x1ffff\n"
      "s_mov_b32 s92, 0\n"
      "s_waitcnt vmcnt(0)\n"
      "Lpoll%=:\n"
      "v_cmp_ne_u32 vcc, s88, v245\n"
      "s_cbranch_vccz Lgot%=\n"
      "global_load_dwordx2 v[244:245], v243, s[36:37] sc1\n"
      "s_add_u32 s92, s92, 1\n"
      "s_cmp_lt_u32 s92, 0x40000\n"
      "s_waitcnt vmcnt(0)\n"
      "s_cbranch_scc1 Lpoll%=\n"
      "Lgot%=:\n"
      "v_readlane_b32 s93, v244, 16\n"
      "s_mov_b64 exec, 0xffff\n"
      "s_nop 2\n"
      "v_add_f32 v249, v188, v244\n"
      "v_mul_f32 v249, 0xbfb8aa3b, v249\n"
      "v_exp_f32 v249, v249\n"
      "s_cmp_eq_u32 s93, s90\n"
      "v_add_f32 v249, 1.0, v249\n"
      "v_rcp_f32 v249, v249\n"
      "s_nop 0\n"
      "v_cvt_pk_bf16_f32 v249, v249, v249\n"
      "global_store_short v248, v249, s[58:59]\n"
      "s_cbranch_scc1 Ldone%=\n"
      "s_mov_b64 exec, 0x1ffff\n"
      "v_mov_b32 v193, s93\n"
      "global_store_dwordx2 v243, v[192:193], s[38:39] sc1\n"
      "Ldone%=:\n"
      "Lend%=:\n"
      "s_mov_b64 exec, -1\n"
      : [tid] "+v"(tid)
      : [ka] "s"(ka), [blk] "s"(blk)
      : "v1", "v2", "v3", "v4", "v5", "v6", "v7", "v8", "v9", "v10", "v11", "v12", "v13", "v14", "v15", "v16",
      "v17", "v18", "v19", "v20", "v21", "v22", "v23", "v24", "v25", "v26", "v27", "v28", "v29", "v30", "v31", "v32",
      "v33", "v34", "v35", "v36", "v37", "v38", "v39", "v40", "v41", "v42", "v43", "v44", "v45", "v46", "v47", "v48",
      "v49", "v50", "v51", "v52", "v53", "v54", "v55", "v56", "v57", "v58", "v59", "v60", "v61", "v62", "v63", "v64",
      "v65", "v66", "v67", "v68", "v69", "v70", "v71", "v72", "v73", "v74", "v75", "v76", "v77", "v78", "v79", "v80",
      "v81", "v82", "v83", "v84", "v85", "v86", "v87", "v88", "v89", "v90", "v91", "v92", "v93", "v94", "v95", "v96",
      "v97", "v98", "v99", "v100", "v101", "v102", "v103", "v104", "v105", "v106", "v107", "v108", "v109", "v110", "v111", "v112",
      "v113", "v114", "v115", "v116", "v117", "v118", "v119", "v120", "v121", "v122", "v123", "v124", "v125", "v126", "v127", "v128",
      "v129", "v130", "v131", "v132", "v133", "v134", "v135", "v136", "v137", "v138", "v139", "v140", "v141", "v142", "v143", "v144",
      "v145", "v146", "v147", "v148", "v149", "v150", "v151", "v152", "v153", "v154", "v155", "v156", "v157", "v158", "v159", "v160",
      "v161", "v162", "v163", "v164", "v165", "v166", "v167", "v168", "v169", "v170", "v171", "v172", "v173", "v174", "v175", "v176",
      "v177", "v178", "v179", "v180", "v181", "v182", "v183", "v184", "v185", "v186", "v187", "v188", "v189", "v190", "v191", "v192",
      "v193", "v194", "v195", "v196", "v197", "v198", "v199", "v200", "v201", "v202", "v203", "v204", "v205", "v206", "v207", "v208",
      "v209", "v210", "v211", "v212", "v213", "v214", "v215", "v216", "v217", "v218", "v219", "v220", "v221", "v222", "v223", "v224",
      "v225", "v226", "v227", "v228", "v229", "v230", "v231", "v232", "v233", "v234", "v235", "v236", "v237", "v238", "v239", "v240",
      "v241", "v242", "v243", "v244", "v245", "v246", "v247", "v248", "v249", "v250", "v251", "v252", "v253", "v254", "v255", "a0",
      "a1", "a2", "a3", "a4", "a5", "a6", "a7", "a8", "a9", "a10", "a11", "a12", "a13", "a14", "a15", "a16",
      "a17", "a18", "a19", "a20", "a21", "a22", "a23", "a24", "a25", "a26", "a27", "a28", "a29", "a30", "a31", "a32",
      "a33", "a34", "a35", "a36", "a37", "a38", "a39", "a40", "a41", "a42", "a43", "a44", "a45", "a46", "a47", "a48",
      "a49", "a50", "a51", "a52", "a53", "a54", "a55", "a56", "a57", "a58", "a59", "a60", "a61", "a62", "a63", "a64",
      "a65", "a66", "a67", "a68", "a69", "a70", "a71", "a72", "a73", "a74", "a75", "a76", "a77", "a78", "a79", "a80",
      "a81", "a82", "a83", "a84", "a85", "a86", "a87", "a88", "a89", "a90", "a91", "a92", "a93", "a94", "a95", "a96",
      "a97", "a98", "a99", "a100", "a101", "a102", "a103", "a104", "a105", "a106", "a107", "a108", "a109", "a110", "a111", "a112",
      "a113", "a114", "a115", "a116", "a117", "a118", "a119", "a120", "a121", "a122", "a123", "a124", "a125", "a126", "a127", "a128",
      "a129", "a130", "a131", "a132", "a133", "a134", "a135", "a136", "a137", "a138", "a139", "a140", "a141", "a142", "a143", "a144",
      "a145", "a146", "a147", "a148", "a149", "a150", "a151", "a152", "a153", "a154", "a155", "a156", "a157", "a158", "a159", "a160",
      "a161", "a162", "a163", "a164", "a165", "a166", "a167", "a168", "a169", "a170", "a171", "a172", "a173", "a174", "a175", "a176",
      "a177", "a178", "a179", "a180", "a181", "a182", "a183", "a184", "a185", "a186", "a187", "a188", "a189", "a190", "a191", "a192",
      "a193", "a194", "a195", "a196", "a197", "a198", "a199", "a200", "a201", "a202", "a203", "a204", "a205", "a206", "a207", "a208",
      "a209", "a210", "a211", "a212", "a213", "a214", "a215", "a216", "a217", "a218", "a219", "a220", "a221", "a222", "a223", "a224",
      "a225", "a226", "a227", "a228", "a229", "a230", "a231", "a232", "a233", "a234", "a235", "a236", "a237", "a238", "a239", "a240",
      "a241", "a242", "a243", "a244", "a245", "a246", "a247", "a248", "a249", "a250", "a251", "a252", "a253", "a254", "a255", "s16",
      "s17", "s18", "s19", "s20", "s21", "s22", "s23", "s24", "s25", "s26", "s27", "s28", "s29", "s30", "s31", "s36",
      "s37", "s38", "s39", "s40", "s41", "s42", "s43", "s44", "s45", "s46", "s47", "s48", "s49", "s50", "s51", "s52",
      "s53", "s54", "s55", "s56", "s57", "s58", "s59", "s60", "s61", "s62", "s63", "s64", "s65", "s66", "s67", "s68",
      "s69", "s70", "s71", "s72", "s73", "s74", "s75", "s76", "s77", "s78", "s79", "s80", "s81", "s82", "s83", "s84",
      "s85", "s86", "s87", "s88", "s89", "s90", "s91", "s92", "s93", "s94", "s95", "s96", "s97", "s98", "s99", "vcc",
      "scc", "memory");
}

extern "C" void kernel_launch(void* const* d_in, const int* in_sizes, int n_in, void* d_out,
                              int out_size, void* d_ws, size_t ws_size, hipStream_t stream) {
  (void)in_sizes; (void)n_in; (void)out_size; (void)ws_size;
  (void)hipFuncSetAttribute((const void*)wd_kernel, hipFuncAttributeMaxDynamicSharedMemorySize, LDS_BYTES);
  wd_kernel<<<256, 256, LDS_BYTES, stream>>>(
      (const int*)d_in[0], (const unsigned short*)d_in[1], (const unsigned short*)d_in[2],
      (const unsigned short*)d_in[3], (const unsigned short*)d_in[4], (const unsigned short*)d_in[5],
      (const unsigned short*)d_in[6], (const unsigned short*)d_in[7], (const unsigned short*)d_in[8],
      (unsigned short*)d_out, (unsigned long long*)d_ws);
}
```
